```python
import torch

def _softmax(x, axis=-1):
    return torch.softmax(x, dim=axis)
def _gelu(x):
    return torch.nn.functional.gelu(x, approximate="tanh")
def _silu(x):
    return torch.nn.functional.silu(x)
def _sigmoid(x):
    return torch.sigmoid(x)
def _layernorm(x, axis=-1, eps=1e-5):
    m = x.mean(dim=axis, keepdim=True)
    v = x.var(dim=axis, keepdim=True, unbiased=False)
    return (x - m) / torch.sqrt(v + eps)
def _scatter_add(tgt, idx, src, axis=0):
    return tgt.clone().index_add_(axis, idx, src)
def _segment_sum(src, seg, n):
    out = torch.zeros((n,) + src.shape[1:], dtype=src.dtype)
    return out.index_add_(0, seg, src)
def _pad(x, pw):
    flat = tuple(v for pair in reversed(pw) for v in pair)
    return torch.nn.functional.pad(x, flat)

kernel_name = 'recurrent_scan__sliding_block_cached__dense_activation_gelu__decode'

def setup_inputs():

    _w = (lambda a: (lambda t: t.to(torch.bfloat16) if t.is_floating_point() else t)(torch.as_tensor(a)))
    def _A_setup():
        B, Sq, D = 1, 1, 512
        Hq, Hkv, Sk, Dh = 32, 8, 32768, 128
        assert Hq % Hkv == 0, (Hq, Hkv)
        return {
            "x": _w((lambda *s: torch.randn(*s, dtype=torch.bfloat16) * 0.4)(B, Sq, D)),
            "k_cache": _w((lambda *s: torch.randn(*s, dtype=torch.bfloat16) * 1.6)(B, Hkv, Sk, Dh)),
            "v_cache": _w((lambda *s: torch.randn(*s, dtype=torch.bfloat16) * 1.6)(B, Hkv, Sk, Dh)),
            "wq": _w((lambda *s: torch.randn(*s, dtype=torch.bfloat16) * 1.6)(D, Hq * Dh) * (D ** -0.5)),
            "wo": _w((lambda *s: torch.randn(*s, dtype=torch.bfloat16) * 1.6)(Hq * Dh, D) * ((Hq * Dh) ** -0.5)),
        }
    _A_in = _A_setup()

    def _B_setup():
        B, S, D, F = 1, 1, 512, 1792
        return {
            "x": _w((lambda *s: torch.randn(*s, dtype=torch.bfloat16) * 0.4)(B, S, D)),
            "w1": _w((lambda *s: torch.randn(*s, dtype=torch.bfloat16) * 1.6)(D, F) * (D ** -0.5)),
            "b1": _w((lambda *s: torch.randn(*s, dtype=torch.bfloat16) * 1.6)(F) * 0.01),
            "w2": _w((lambda *s: torch.randn(*s, dtype=torch.bfloat16) * 1.6)(F, D) * (F ** -0.5)),
            "b2": _w((lambda *s: torch.randn(*s, dtype=torch.bfloat16) * 1.6)(D) * 0.01),
        }
    _B_in = _B_setup()

    return {
        "A_x": _A_in["x"],
        "A_k_cache": _A_in["k_cache"],
        "A_v_cache": _A_in["v_cache"],
        "A_wq": _A_in["wq"],
        "A_wo": _A_in["wo"],
        "B_x": _B_in["x"],
        "B_w1": _B_in["w1"],
        "B_b1": _B_in["b1"],
        "B_w2": _B_in["w2"],
        "B_b2": _B_in["b2"],
    }

def reference(A_x, A_k_cache, A_v_cache, A_wq, A_wo, B_x, B_w1, B_b1, B_w2, B_b2):
    def _A_ref(x, k_cache, v_cache, wq, wo):
        B, Sq, D = x.shape
        Hq, Dh = 32, 128
        Hkv, Sk = k_cache.shape[1], k_cache.shape[2]
        W = min(4096, max(256, Sk // 4))
        rep = Hq // Hkv
        q = (lambda x, *d: x.permute(*d))((x @ wq).reshape(B, Sq, Hq, Dh), 0, 2, 1, 3)
        k = (lambda x, n, axis: torch.repeat_interleave(x, n, dim=axis))(k_cache[:, :, Sk - W:, :], rep, axis=1)
        v = (lambda x, n, axis: torch.repeat_interleave(x, n, dim=axis))(v_cache[:, :, Sk - W:, :], rep, axis=1)
        scores = torch.einsum("bhqd,bhkd->bhqk", q, k) * (Dh ** -0.5)
        p = _softmax(scores, axis=-1)
        o = torch.einsum("bhqk,bhkd->bhqd", p, v)
        return (lambda x, *d: x.permute(*d))(o, 0, 2, 1, 3).reshape(B, Sq, Hq * Dh) @ wo

    def _B_ref(x, w1, b1, w2, b2):
        h = torch.einsum("bsd,df->bsf", x, w1) + b1
        h = _gelu(h)
        return torch.einsum("bsf,fd->bsd", h, w2) + b2

    x = A_x
    xn = _layernorm(x, axis=-1)
    s = _A_ref(xn, A_k_cache, A_v_cache, A_wq, A_wo)
    g = _silu(_B_ref(xn, B_w1, B_b1, B_w2, B_b2))
    return x + s * g
```

```cpp
#include <hip/hip_runtime.h>
#include <hip/hip_bf16.h>
#include <stdint.h>

typedef unsigned short u16;
__device__ __forceinline__ float bf2f(u16 b) { return __uint_as_float(((unsigned)b) << 16); }
__device__ __forceinline__ float rbf(float f) { return __bfloat162float(__float2bfloat16(f)); }

constexpr int D = 512, HQ = 32, HKV = 8, SK = 32768, DH = 128, W = 4096, F = 1792;
constexpr int NSPLIT = 32, KPS = W / NSPLIT;

__device__ void layernorm_block(const u16* x, float* xn_s, float* red) {
  int t = threadIdx.x;
  float a = bf2f(x[t]), b = bf2f(x[t + 256]);
  float s = a + b;
  for (int o = 32; o > 0; o >>= 1) s += __shfl_xor(s, o);
  if ((t & 63) == 0) red[t >> 6] = s;
  __syncthreads();
  float mean = (red[0] + red[1] + red[2] + red[3]) * (1.f / 512.f);
  __syncthreads();
  float da = a - mean, db = b - mean;
  float v = da * da + db * db;
  for (int o = 32; o > 0; o >>= 1) v += __shfl_xor(v, o);
  if ((t & 63) == 0) red[t >> 6] = v;
  __syncthreads();
  float var = (red[0] + red[1] + red[2] + red[3]) * (1.f / 512.f);
  float mb = rbf(mean), vb = rbf(var);
  float den = rbf(sqrtf(rbf(vb + 1e-5f)));
  xn_s[t] = rbf(rbf(a - mb) / den);
  xn_s[t + 256] = rbf(rbf(b - mb) / den);
  __syncthreads();
}

__global__ void __launch_bounds__(256) k1(const u16* x, const u16* wq, const u16* w1, const u16* b1, float* q, float* h) {
  __shared__ float xn[512]; __shared__ float red[4]; __shared__ float part[4][64];
  layernorm_block(x, xn, red);
  int b = blockIdx.x, t = threadIdx.x, c = t & 63, r = t >> 6;
  const u16* wsrc; int ld, col;
  if (b < 64) { wsrc = wq; ld = 4096; col = b * 64 + c; } else { wsrc = w1; ld = F; col = (b - 64) * 64 + c; }
  float acc = 0.f;
  for (int k = r * 128; k < r * 128 + 128; ++k) acc += xn[k] * bf2f(wsrc[(size_t)k * ld + col]);
  part[r][c] = acc;
  __syncthreads();
  if (t < 64) {
    float s = part[0][c] + part[1][c] + part[2][c] + part[3][c];
    if (b < 64) q[col] = rbf(s);
    else {
      float u = rbf(rbf(s) + bf2f(b1[col]));
      float g = 0.5f * u * (1.f + tanhf(0.7978845608028654f * (u + 0.044715f * u * u * u)));
      h[col] = rbf(g);
    }
  }
}

__global__ void __launch_bounds__(256) k2(const u16* kc, const u16* vc, const float* q, float* pm, float* pl, float* po) {
  __shared__ float qs[4][128]; __shared__ float sc[4][KPS]; __shared__ float mx[4];
  int kvh = blockIdx.x & 7, sp = blockIdx.x >> 3, t = threadIdx.x;
  for (int i = t; i < 512; i += 256) qs[i >> 7][i & 127] = q[kvh * 512 + i];
  __syncthreads();
  const u16* kb = kc + ((size_t)kvh * SK + (SK - W) + sp * KPS) * DH;
  const u16* vb = vc + ((size_t)kvh * SK + (SK - W) + sp * KPS) * DH;
  for (int i = t; i < 4 * KPS; i += 256) {
    int hh = i / KPS, key = i % KPS;
    float acc = 0.f;
    for (int d = 0; d < 128; ++d) acc += qs[hh][d] * bf2f(kb[key * 128 + d]);
    sc[hh][key] = rbf(rbf(acc) * 0.08838834764831845f);
  }
  __syncthreads();
  if (t < 4) { float m = -INFINITY; for (int k = 0; k < KPS; ++k) m = fmaxf(m, sc[t][k]); mx[t] = m; float l = 0.f; for (int k = 0; k < KPS; ++k) { float p = __expf(sc[t][k] - m); sc[t][k] = p; l += p; }
    pm[(kvh * 4 + t) * NSPLIT + sp] = m; pl[(kvh * 4 + t) * NSPLIT + sp] = l; }
  __syncthreads();
  for (int i = t; i < 512; i += 256) {
    int hh = i >> 7, d = i & 127; float acc = 0.f;
    for (int k = 0; k < KPS; ++k) acc += sc[hh][k] * bf2f(vb[k * 128 + d]);
    po[((size_t)(kvh * 4 + hh) * NSPLIT + sp) * 128 + d] = acc;
  }
}

__global__ void __launch_bounds__(256) k3(const float* pm, const float* pl, const float* po, const u16* wo, const float* h, const u16* w2, float* sp_, float* yp) {
  __shared__ float o[128];
  int b = blockIdx.x, t = threadIdx.x;
  if (b < 32) {
    if (t < 128) {
      float M = -INFINITY; for (int s = 0; s < NSPLIT; ++s) M = fmaxf(M, pm[b * NSPLIT + s]);
      float L = 0.f, acc = 0.f;
      for (int s = 0; s < NSPLIT; ++s) { float w = __expf(pm[b * NSPLIT + s] - M); L += w * pl[b * NSPLIT + s]; acc += w * po[((size_t)b * NSPLIT + s) * 128 + t]; }
      o[t] = rbf(acc / L);
    }
    __syncthreads();
    for (int c = t; c < 512; c += 256) { float acc = 0.f; for (int r = 0; r < 128; ++r) acc += o[r] * bf2f(wo[(size_t)(b * 128 + r) * 512 + c]); sp_[b * 512 + c] = acc; }
  } else {
    int bb = b - 32;
    for (int c = t; c < 512; c += 256) { float acc = 0.f; for (int r = 0; r < 128; ++r) acc += h[bb * 128 + r] * bf2f(w2[(size_t)(bb * 128 + r) * 512 + c]); yp[bb * 512 + c] = acc; }
  }
}

__global__ void __launch_bounds__(512) k4(const u16* x, const float* sp_, const float* yp, const u16* b2, __hip_bfloat16* out) {
  int c = threadIdx.x;
  float s = 0.f; for (int i = 0; i < 32; ++i) s += sp_[i * 512 + c];
  float y = 0.f; for (int i = 0; i < 14; ++i) y += yp[i * 512 + c];
  s = rbf(s); y = rbf(rbf(y) + bf2f(b2[c]));
  float g = rbf(y / (1.f + __expf(-y)));
  out[c] = __float2bfloat16(bf2f(x[c]) + rbf(s * g));
}

extern "C" void kernel_launch(void* const* d_in, const int* in_sizes, int n_in, void* d_out, int out_size, void* d_ws, size_t ws_size, hipStream_t stream) {
  const u16* x = (const u16*)d_in[0]; const u16* kc = (const u16*)d_in[1]; const u16* vc = (const u16*)d_in[2];
  const u16* wq = (const u16*)d_in[3]; const u16* wo = (const u16*)d_in[4];
  const u16* w1 = (const u16*)d_in[6]; const u16* b1 = (const u16*)d_in[7]; const u16* w2 = (const u16*)d_in[8]; const u16* b2 = (const u16*)d_in[9];
  float* ws = (float*)d_ws;
  float* q = ws; float* h = q + 4096; float* pm = h + 2048; float* pl = pm + 1024; float* po = pl + 1024; float* sp_ = po + 32 * 32 * 128; float* yp = sp_ + 32 * 512;
  k1<<<64 + 28, 256, 0, stream>>>(x, wq, w1, b1, q, h);
  k2<<<256, 256, 0, stream>>>(kc, vc, q, pm, pl, po);
  k3<<<32 + 14, 256, 0, stream>>>(pm, pl, po, wo, h, w2, sp_, yp);
  k4<<<1, 512, 0, stream>>>(x, sp_, yp, b2, (__hip_bfloat16*)d_out);
}
```

```cpp
#include <hip/hip_runtime.h>
#include <hip/hip_bf16.h>
#include <stdint.h>

typedef unsigned u32;
typedef unsigned short u16;
typedef unsigned long long u64;
typedef u32 v4u __attribute__((ext_vector_type(4)));
typedef u32 v2u __attribute__((ext_vector_type(2)));
typedef float v4f __attribute__((ext_vector_type(4)));
typedef float v2f __attribute__((ext_vector_type(2)));
typedef short v4s __attribute__((ext_vector_type(4)));
typedef short v8s __attribute__((ext_vector_type(8)));
typedef __bf16 v8bf __attribute__((ext_vector_type(8)));
typedef __bf16 v2bf __attribute__((ext_vector_type(2)));
#define LAS __attribute__((address_space(3)))
#define GAS __attribute__((address_space(1)))
#define DEVI __device__ __forceinline__

#define Q_WAIT 4
#define DLY2 8
#define DLY3 8
#define STAMPS 0

constexpr int L_V = 0, L_WO = 32768, L_W2 = 49152, L_WQ = 57344, L_W1 = 73728, L_XN = 81920, L_QF = 83968;
constexpr int L_P = 84992, P_STR = 288, L_ML = L_P + 4 * P_STR, L_OP = L_ML + 128, L_ML2 = L_OP + 512, L_H = L_ML2 + 32, L_FLAG = L_H + 64;
constexpr int LDS_BYTES = L_FLAG + 16;
constexpr u32 W_E = 0, W_Q = 4096, W_H = W_Q + 32768, W_O = W_H + 32768, W_S = W_O + 524288, W_END = W_S + 262144;
constexpr u32 W_ST = 1u << 21;
constexpr u32 OOB = 0x7fffffffu;
constexpr int SPIN_CAP = 1 << 16;
constexpr float LOG2E = 1.4426950408889634f;

DEVI float asf(u32 x) { return __builtin_bit_cast(float, x); }
DEVI u32 asu(float x) { return __builtin_bit_cast(u32, x); }
DEVI u32 pkbf(float lo, float hi) { v2f p = {lo, hi}; return __builtin_bit_cast(u32, __builtin_convertvector(p, v2bf)); }
DEVI float rbf(float x) { return asf(pkbf(x, x) << 16); }
DEVI float bflo(u32 w) { return asf(w << 16); }
DEVI float bfhi(u32 w) { return asf(w & 0xffff0000u); }
template <int CTRL> DEVI u32 dppu(u32 x) { return (u32)__builtin_amdgcn_mov_dpp((int)x, CTRL, 0xf, 0xf, true); }
template <int CTRL> DEVI float dppf(float x) { return asf(dppu<CTRL>(asu(x))); }
constexpr int XOR1 = 0xB1, XOR2 = 0x4E, ROT1 = 0x39, ROT2 = 0x4E, ROT3 = 0x93, HMIR = 0x141, ROR4 = 0x124, ROR8 = 0x128;
DEVI float sum16(float x) { auto s = __builtin_amdgcn_permlane16_swap(asu(x), asu(x), false, false); return asf(s[0]) + asf(s[1]); }
DEVI float sum32(float x) { auto s = __builtin_amdgcn_permlane32_swap(asu(x), asu(x), false, false); return asf(s[0]) + asf(s[1]); }
DEVI float max16(float x) { auto s = __builtin_amdgcn_permlane16_swap(asu(x), asu(x), false, false); return fmaxf(asf(s[0]), asf(s[1])); }
DEVI float max32(float x) { auto s = __builtin_amdgcn_permlane32_swap(asu(x), asu(x), false, false); return fmaxf(asf(s[0]), asf(s[1])); }
DEVI float tsum16(float a, float b) { auto s = __builtin_amdgcn_permlane16_swap(asu(a), asu(b), false, false); return asf(s[0]) + asf(s[1]); }
DEVI float tsum32(float a, float b) { auto s = __builtin_amdgcn_permlane32_swap(asu(a), asu(b), false, false); return asf(s[0]) + asf(s[1]); }
DEVI float wave_sum(float x) { x += dppf<XOR1>(x); x += dppf<XOR2>(x); x += dppf<HMIR>(x); x += dppf<ROR8>(x); return sum32(sum16(x)); }
DEVI v4f mfma(v4u a, v4u b, v4f c) { return __builtin_amdgcn_mfma_f32_16x16x32_bf16(__builtin_bit_cast(v8bf, a), __builtin_bit_cast(v8bf, b), c, 0, 0, 0); }
DEVI v4u trfrag(LAS char* p0, LAS char* p1) {
  v4s a = __builtin_amdgcn_ds_read_tr16_b64_v4i16((LAS v4s*)p0), b = __builtin_amdgcn_ds_read_tr16_b64_v4i16((LAS v4s*)p1);
  return __builtin_bit_cast(v4u, __builtin_shufflevector(a, b, 0, 1, 2, 3, 4, 5, 6, 7));
}
DEVI v2u pack24(float a, float b, u32 tag) { return v2u{__builtin_amdgcn_perm(asu(a), asu(b), 0x07060501u), __builtin_amdgcn_perm(asu(b), tag, 0x07060100u)}; }
DEVI float un24a(u32 d0) { return asf(d0 & 0xffffff00u); }
DEVI float un24b(u32 d0, u32 d1) { return asf(__builtin_amdgcn_perm(d1, d0, 0x0706000cu)); }

#define BAR() asm volatile("s_waitcnt lgkmcnt(0)\n\ts_barrier" ::: "memory")
#define DMA4(sb, m0v, v0, v1, v2, v3)                                                                                   \
  asm volatile("s_mov_b32 m0, %1\n\ts_nop 0\n\tglobal_load_lds_dwordx4 %2, %0 nt\n\tglobal_load_lds_dwordx4 %3, %0 offset:1024 nt\n\t" \
               "global_load_lds_dwordx4 %4, %0 offset:2048 nt\n\tglobal_load_lds_dwordx4 %5, %0 offset:3072 nt"           \
               :: "s"(sb), "s"(m0v), "v"(v0), "v"(v1), "v"(v2), "v"(v3) : "memory")
#define DMA3(sb, m0v, v0, v1, v2)                                                                                       \
  asm volatile("s_mov_b32 m0, %1\n\ts_nop 0\n\tglobal_load_lds_dwordx4 %2, %0 nt\n\tglobal_load_lds_dwordx4 %3, %0 offset:1024 nt\n\t" \
               "global_load_lds_dwordx4 %4, %0 offset:2048 nt" :: "s"(sb), "s"(m0v), "v"(v0), "v"(v1), "v"(v2) : "memory")

#define STAMP(i)
#define STAMP_OUT()

template <int N>
DEVI void gather(__amdgpu_buffer_rsrc_t rs, u32 (&off)[N], v4u (&v)[N], u32 tagA, u32 tagB) {
#pragma unroll
  for (int i = 0; i < N; ++i) v[i] = v4u{0u, 0u, 0u, 0u};
  for (int it = 0; it < SPIN_CAP; ++it) {
    asm volatile("" ::: "memory");
    v4u n[N];
#pragma unroll
    for (int i = 0; i < N; ++i) n[i] = __builtin_amdgcn_raw_buffer_load_b128(rs, off[i], 0, 16);
    bool ok = true;
#pragma unroll
    for (int i = 0; i < N; ++i) {
      const bool good = ((((n[i].y ^ tagA) | (n[i].w ^ tagB)) & 0xffffu) == 0u) && off[i] != OOB;
      if (good) { v[i] = n[i]; off[i] = OOB; }
      ok = ok && off[i] == OOB;
    }
    if (__all(ok)) break;
  }
}

__global__ void __launch_bounds__(320)
fused_decode(const char* __restrict__ x, char* ws, const char* __restrict__ wq, const char* __restrict__ w1, const char* __restrict__ kw,
             const char* __restrict__ vw, const char* __restrict__ wo, const char* __restrict__ w2, const char* __restrict__ b1,
             const char* __restrict__ b2, u16* __restrict__ out) {
  __shared__ __attribute__((aligned(16))) char smem[LDS_BYTES];
  LAS char* sm = (LAS char*)smem;
  const u32 smb = (u32)(uintptr_t)sm;
  const int b = blockIdx.x, G = b & 7, j = b >> 3;
  const int lane = threadIdx.x & 63, wave = __builtin_amdgcn_readfirstlane(threadIdx.x >> 6);
  STAMP(0);
  v4u xr; u32 ev;
  asm volatile("global_load_dwordx4 %0, %2, %3\n\tglobal_load_dword %1, %4, %5 sc1"
               : "=&v"(xr), "=&v"(ev) : "v"(lane * 16), "s"(x), "v"(0), "s"(ws) : "memory");
  const bool proj = wave == 0 || (wave == 1 && j < 28);
  if (proj) {
    const u32 rs = wave == 0 ? 8192u : 3584u;
    const char* base = wave == 0 ? wq + (size_t)((j >> 4) * 256) * 8192 + G * 1024 + (j & 15) * 64 : w1 + (size_t)((j & 3) * 128) * 3584 + G * 448 + (j >> 2) * 64;
    const u32 v0 = (lane >> 2) * rs + (lane & 3) * 16, st = 16 * rs - 1024;
    const u32 ld = smb + (wave == 0 ? L_WQ : L_W1);
    DMA4(base, ld, v0, v0 + st, v0 + 2 * st, v0 + 3 * st);
    DMA4(base + 64 * rs, ld + 4096, v0, v0 + st, v0 + 2 * st, v0 + 3 * st);
    if (wave == 0) {
      DMA4(base + 128 * 8192, ld + 8192, v0, v0 + st, v0 + 2 * st, v0 + 3 * st);
      DMA4(base + 192 * 8192, ld + 12288, v0, v0 + st, v0 + 2 * st, v0 + 3 * st);
    }
  } else if (wave == 2) {
    if (lane == 0) *(LAS u32*)(sm + L_FLAG) = 0u;
  } else if (wave == 3) {
    *(LAS v4u*)(sm + L_W2 + 7168 + lane * 16) = v4u{0u, 0u, 0u, 0u};
  }
  BAR();
  const __amdgpu_buffer_rsrc_t wsr = __builtin_amdgcn_make_buffer_rsrc(ws, 0, W_END, 0x00020000);
  const int g4 = lane >> 4, c16 = lane & 15;
  const int rbl = j >> 2, cb = j & 3;
  if (wave == 4) {
    const u32 sw = 16 * (c16 ^ (g4 << 2));
    asm volatile("s_barrier" ::: "memory");
    {
      const char* base = vw + ((size_t)G * 32768 + j * 128) * 256;
      const u32 v0 = g4 * 256 + sw;
#pragma unroll
      for (int a = 0; a < 8; ++a) DMA4(base + a * 4096, smb + L_V + a * 4096, v0, v0 ^ 16, v0 ^ 32, v0 ^ 48);
    }
    STAMP(1);
    {
      const char* base = wo + (size_t)(G * 512 + rbl * 64) * 1024 + cb * 256;
      const u32 v0 = g4 * 1024 + sw;
#pragma unroll
      for (int a = 0; a < 4; ++a) DMA4(base + a * 16384, smb + L_WO + a * 4096, v0, (v0 ^ 16) + 3072, (v0 ^ 32) + 6144, (v0 ^ 48) + 9216);
    }
    const int hid0 = G * 224 + (rbl & 3) * 8 + (rbl >> 2) * 2;
    {
      const char* base = w2 + (size_t)hid0 * 1024 + cb * 256;
      const u32 v0 = ((g4 & 1) + 4 * (g4 >> 1)) * 1024 + sw;
      DMA4(base, smb + L_W2, v0, (v0 ^ 16) + 31744, (v0 ^ 32) + 2 * 31744, (v0 ^ 48) + 3 * 31744);
      DMA3(base + 4 * 32768, smb + L_W2 + 4096, v0, (v0 ^ 16) + 31744, (v0 ^ 32) + 2 * 31744);
    }
    STAMP(2);
    asm volatile("s_waitcnt vmcnt(23)" : "+v"(ev) :: "memory");
    STAMP(3);
    asm volatile("s_barrier" ::: "memory");
    const u32 e = __builtin_amdgcn_readfirstlane(ev), tagA = (e + 1u) & 0xffffu, tagB = ((e >> 16) + 1u) & 0xffffu;
    const int cbk = lane & 7;
    const bool hl = lane < 32 && cbk < 7;
    const int ck = cbk < 7 ? cbk : 6;
    const u32 ba = *(const GAS u32*)(b1 + (hid0 + 32 * ck) * 2), bb = *(const GAS u32*)(b1 + (hid0 + 32 * ck + 4) * 2);
    u32 off[1] = {hl ? W_H + (u32)(((G * 4 + (lane >> 3)) * 8 + cbk) * 128 + rbl * 16) : OOB};
    v4u u[1];
    gather<1>(wsr, off, u, tagA, tagB);
    float h0 = un24a(u[0].x), h4 = un24b(u[0].x, u[0].y), h1 = un24a(u[0].z), h5 = un24b(u[0].z, u[0].w);
    h0 += dppf<ROR8>(h0); h1 += dppf<ROR8>(h1); h4 += dppf<ROR8>(h4); h5 += dppf<ROR8>(h5);
    float hv[4] = {sum16(h0), sum16(h1), sum16(h4), sum16(h5)};
    const float bv[4] = {bflo(ba), bfhi(ba), bflo(bb), bfhi(bb)};
#pragma unroll
    for (int k = 0; k < 4; ++k) {
      const float t = rbf(rbf(hv[k]) + bv[k]);
      const float z = t * (1.5957691216057308f * LOG2E) * fmaf(0.044715f * t, t, 1.0f);
      hv[k] = t * __builtin_amdgcn_rcpf(1.0f + __builtin_amdgcn_exp2f(-z));
    }
    v2u hp = {pkbf(hv[0], hv[1]), pkbf(hv[2], hv[3])};
    if (cbk == 7) hp = v2u{0u, 0u};
    if (lane < 8) *(LAS v2u*)(sm + L_H + lane * 8) = hp;
    asm volatile("s_waitcnt vmcnt(0)" ::: "memory");
    STAMP(4);
    BAR();
    STAMP_OUT();
    return;
  }
  v4u kf[8];
  {
    const char* kb = kw + ((size_t)G * 32768 + j * 128 + wave * 32) * 256 + (((lane >> 2) & 3) * 2048 + g4 * 64 + (lane & 3) * 16);
#pragma unroll
    for (int t = 0; t < 8; ++t) kf[t] = __builtin_nontemporal_load((const GAS v4u*)(kb + t * 256));
  }
  asm volatile("s_barrier" ::: "memory");
  STAMP(1);
  if (wave == 0) asm volatile("s_waitcnt vmcnt(24)" ::: "memory");
  else if (proj) asm volatile("s_waitcnt vmcnt(16)" ::: "memory");
  else asm volatile("s_waitcnt vmcnt(8)" ::: "memory");
  asm volatile("" : "+v"(xr), "+v"(ev));
  const u32 e = __builtin_amdgcn_readfirstlane(ev), tagA = (e + 1u) & 0xffffu, tagB = ((e >> 16) + 1u) & 0xffffu;
  const u32 tagL = (lane & 1) ? tagB : tagA;
  if (proj) {
    float xv[8];
#pragma unroll
    for (int k = 0; k < 4; ++k) { xv[2 * k] = bflo(xr[k]); xv[2 * k + 1] = bfhi(xr[k]); }
    const float mean = wave_sum(((xv[0] + xv[1]) + (xv[2] + xv[3])) + ((xv[4] + xv[5]) + (xv[6] + xv[7]))) * (1.0f / 512.0f);
    float sq = 0.f;
#pragma unroll
    for (int k = 0; k < 8; ++k) { const float d = xv[k] - mean; sq = fmaf(d, d, sq); }
    const float var = wave_sum(sq) * (1.0f / 512.0f);
    const float mb = rbf(mean), den = rbf(__builtin_sqrtf(rbf(rbf(var) + 1e-5f)));
    v4u xn;
#pragma unroll
    for (int k = 0; k < 4; ++k) xn[k] = pkbf(rbf(xv[2 * k] - mb) / den, rbf(xv[2 * k + 1] - mb) / den);
    LAS char* xs = sm + L_XN + wave * 1024;
    *(LAS v4u*)(xs + lane * 16) = xn;
    const int nrep = wave == 0 ? 2 : 1;
    LAS char* ap = xs + (wave == 0 ? (j >> 4) * 512 : (j & 3) * 256) + g4 * 16;
    LAS char* bp = sm + (wave == 0 ? L_WQ : L_W1) + (8 * g4 + (c16 >> 2)) * 64 + (lane & 3) * 16;
    STAMP(2);
    asm volatile("s_waitcnt vmcnt(8)" ::: "memory");
    STAMP(3);
    v4f a0 = {0.f, 0.f, 0.f, 0.f}, a1 = a0;
    for (int r = 0; r < nrep; ++r) {
#pragma unroll
      for (int ks = 0; ks < 4; ++ks) {
        const v4u af = *(LAS v4u*)(ap + ks * 64);
        a0 = mfma(af, trfrag(bp + ks * 2048, bp + ks * 2048 + 256), a0);
        a1 = mfma(af, trfrag(bp + ks * 2048 + 8, bp + ks * 2048 + 264), a1);
      }
      ap += 256; bp += 8192;
    }
    const v2u d = pack24(a0[0], a1[0], tagL);
    const v4u unit = {d.x, d.y, dppu<XOR1>(d.x), dppu<XOR1>(d.y)};
    const u32 dst = wave == 0 ? W_Q + (u32)(G * 32 + j) * 128 : W_H + (u32)((G * 4 + (j & 3)) * 8 + (j >> 2)) * 128;
    const u32 so = (lane < 16 && !(lane & 1)) ? dst + ((lane >> 1) & 1) * 64 + (lane >> 2) * 16 : OOB;
    __builtin_amdgcn_raw_buffer_store_b128(unit, wsr, so, 0, 16);
    STAMP(4);
  }
  v4u qf;
  if (wave == 2) {
    asm volatile("s_waitcnt vmcnt(%0)" :: "i"(Q_WAIT) : "memory");
    STAMP(2);
    const u32 o0 = W_Q + (u32)(G * 32 + (c16 & 12) + g4) * 128 + (lane & 3) * 16;
    u32 off[4] = {o0, o0 + 64, o0 + 2048, o0 + 2048 + 64};
    v4u u[4];
    gather<4>(wsr, off, u, tagA, tagB);
    qf.x = pkbf(un24a(u[0].x) + un24a(u[2].x), un24a(u[0].z) + un24a(u[2].z));
    qf.y = pkbf(un24a(u[1].x) + un24a(u[3].x), un24a(u[1].z) + un24a(u[3].z));
    qf.z = pkbf(un24b(u[0].x, u[0].y) + un24b(u[2].x, u[2].y), un24b(u[0].z, u[0].w) + un24b(u[2].z, u[2].w));
    qf.w = pkbf(un24b(u[1].x, u[1].y) + un24b(u[3].x, u[3].y), un24b(u[1].z, u[1].w) + un24b(u[3].z, u[3].w));
    *(LAS v4u*)(sm + L_QF + lane * 16) = qf;
    asm volatile("s_waitcnt lgkmcnt(0)" ::: "memory");
    if (lane == 0) *(volatile LAS u32*)(sm + L_FLAG) = 1u;
    STAMP(3);
  } else {
    for (int it = 0; it < (SPIN_CAP << 6); ++it) {
      if (*(volatile LAS u32*)(sm + L_FLAG) != 0u) break;
      __builtin_amdgcn_s_sleep(1);
    }
    asm volatile("" ::: "memory");
    qf = *(LAS v4u*)(sm + L_QF + lane * 16);
  }
  STAMP(5);
  {
    v4f d[8];
#pragma unroll
    for (int t = 0; t < 8; ++t) d[t] = mfma(kf[t], qf, v4f{0.f, 0.f, 0.f, 0.f});
    const int u4 = lane & 3;
    float sc[2];
#pragma unroll
    for (int T = 0; T < 2; ++T) {
      float z[4];
#pragma unroll
      for (int r = 0; r < 4; ++r) {
        const float e0 = d[2 * ((0 - r) & 3) + T][0], e1 = d[2 * ((1 - r) & 3) + T][1], e2 = d[2 * ((2 - r) & 3) + T][2], e3 = d[2 * ((3 - r) & 3) + T][3];
        z[r] = u4 == 0 ? e0 : u4 == 1 ? e1 : u4 == 2 ? e2 : e3;
      }
      const float raw = (z[0] + dppf<ROT1>(z[1])) + (dppf<ROT2>(z[2]) + dppf<ROT3>(z[3]));
      sc[T] = rbf(rbf(raw) * 0.08838834764831845f);
    }
    float m = fmaxf(sc[0], sc[1]);
    m = fmaxf(m, dppf<XOR1>(m)); m = fmaxf(m, dppf<XOR2>(m)); m = max32(max16(m));
    const u32 pp = pkbf(__builtin_amdgcn_exp2f((sc[0] - m) * LOG2E), __builtin_amdgcn_exp2f((sc[1] - m) * LOG2E));
    const int hh = (lane >> 2) & 3;
    *(LAS u32*)(sm + L_P + hh * P_STR + (wave * 16 + g4 * 4 + u4) * 4) = pp;
    float l = bflo(pp) + bfhi(pp);
    l += dppf<XOR1>(l); l += dppf<XOR2>(l); l = sum32(sum16(l));
    if (lane < 16 && u4 < 2) *(LAS float*)(sm + L_ML + hh * 32 + u4 * 16 + wave * 4) = u4 ? l : m;
  }
  STAMP(6);
  BAR();
  STAMP(7);
  const u32 trsw = ((c16 >> 2) << 2);
  LAS char* tb[2][2];
#pragma unroll
  for (int bk = 0; bk < 2; ++bk)
#pragma unroll
    for (int t = 0; t < 2; ++t)
      tb[bk][t] = sm + (8 * g4 + 4 * t + (c16 >> 2)) * 256 + 16 * ((2 * (2 * wave + bk) + ((lane >> 1) & 1)) ^ (trsw | ((2 * g4 + t) & 3))) + 8 * (lane & 1);
  {
    const v4f mm = *(LAS v4f*)(sm + L_ML + g4 * 32), ll = *(LAS v4f*)(sm + L_ML + g4 * 32 + 16);
    v4f d0[4], d1[4];
#pragma unroll
    for (int s = 0; s < 4; ++s) {
      const v4u af = *(LAS v4u*)(sm + L_P + (c16 >> 2) * P_STR + s * 64 + g4 * 16);
      d0[s] = mfma(af, trfrag(tb[0][0] + L_V + s * 8192, tb[0][1] + L_V + s * 8192), v4f{0.f, 0.f, 0.f, 0.f});
      d1[s] = mfma(af, trfrag(tb[1][0] + L_V + s * 8192, tb[1][1] + L_V + s * 8192), v4f{0.f, 0.f, 0.f, 0.f});
    }
    const float M = fmaxf(fmaxf(mm[0], mm[1]), fmaxf(mm[2], mm[3]));
    float L = 0.f, o0 = 0.f, o1 = 0.f;
#pragma unroll
    for (int s = 0; s < 4; ++s) {
      const float w = __builtin_amdgcn_exp2f((mm[s] - M) * LOG2E);
      L = fmaf(w, ll[s], L); o0 = fmaf(w, d0[s][0], o0); o1 = fmaf(w, d1[s][0], o1);
    }
    const float inv = __builtin_amdgcn_rcpf(L);
    const u32 lse = asu(fmaf(M, LOG2E, __builtin_amdgcn_logf(L)));
    const u32 da = pkbf(o0 * inv, o1 * inv), db = ((lane & 1) ? (lse << 16) : (lse & 0xffff0000u)) | tagL;
    const v4u unit = {da, db, dppu<XOR1>(da), dppu<XOR1>(db)};
    const u32 so = (lane & 1) ? OOB : W_O + (u32)((((G * 4 + g4) * 4 + wave) * 32 + j) * 128 + (c16 >> 1) * 16);
    __builtin_amdgcn_raw_buffer_store_b128(unit, wsr, so, 0, 16);
  }
  STAMP(8);
  const int f = ((b & 7) << 3) | (b >> 3);
  const int U = 4 * (f & 63) + (lane & 3), col = 32 * (U >> 4) + (U & 15) + 16 * (lane >> 5);
  u32 xc = 0, bc = 0;
  if (b < 64 && wave == 0) { xc = *(const GAS u16*)(x + col * 2); bc = *(const GAS u16*)(b2 + col * 2); }
  BAR();
  v4u wf[2][2]; v4f y0, y1;
  {
#pragma unroll
    for (int ks = 0; ks < 2; ++ks)
#pragma unroll
      for (int bk = 0; bk < 2; ++bk) wf[ks][bk] = trfrag(tb[bk][0] + L_WO + ks * 8192, tb[bk][1] + L_WO + ks * 8192);
    const v4u hf = *(LAS v4u*)(sm + L_H + g4 * 16);
    y0 = mfma(hf, trfrag(tb[0][0] + L_W2, tb[0][1] + L_W2), v4f{0.f, 0.f, 0.f, 0.f});
    y1 = mfma(hf, trfrag(tb[1][0] + L_W2, tb[1][1] + L_W2), v4f{0.f, 0.f, 0.f, 0.f});
  }
  {
    if (DLY2) __builtin_amdgcn_s_sleep(DLY2);
    const u32 o0 = W_O + (u32)(((G * 4 + (rbl >> 1)) * 4 + (rbl & 1) * 2) * 4096 + wave * 1024 + lane * 16);
    u32 off[2] = {o0, o0 + 4096};
    v4u u[2];
    gather<2>(wsr, off, u, tagA, tagB);
    STAMP(9);
    const float lse = asf(__builtin_amdgcn_perm(u[0].y, u[0].w, 0x07060302u));
    float M = fmaxf(lse, dppf<ROR8>(lse)); M = max32(max16(M));
    const float w = __builtin_amdgcn_exp2f(lse - M);
    float L = w + dppf<ROR8>(w); L = sum32(sum16(L));
    const float t0 = tsum32(w * bflo(u[0].x), w * bflo(u[1].x)), t1 = tsum32(w * bfhi(u[0].x), w * bfhi(u[1].x));
    const float t2 = tsum32(w * bflo(u[0].z), w * bflo(u[1].z)), t3 = tsum32(w * bfhi(u[0].z), w * bfhi(u[1].z));
    float r0 = tsum16(t0, t1), r1 = tsum16(t2, t3);
    r0 += dppf<ROR8>(r0); r1 += dppf<ROR8>(r1);
    *(LAS u32*)(sm + L_OP + wave * 128 + (g4 * 8 + (lane & 7)) * 4) = pkbf(r0, r1);
    if (lane < 2) *(LAS float*)(sm + L_ML2 + lane * 16 + wave * 4) = lane ? L : M;
  }
  BAR();
  {
    const v4f mm = *(LAS v4f*)(sm + L_ML2), ll = *(LAS v4f*)(sm + L_ML2 + 16);
    const v4u a0 = *(LAS v4u*)(sm + L_OP + (lane & 3) * 128 + g4 * 16), a1 = *(LAS v4u*)(sm + L_OP + (lane & 3) * 128 + 64 + g4 * 16);
    v4f s0 = mfma(a0, wf[0][0], v4f{0.f, 0.f, 0.f, 0.f}), s1 = mfma(a0, wf[0][1], v4f{0.f, 0.f, 0.f, 0.f});
    s0 = mfma(a1, wf[1][0], s0); s1 = mfma(a1, wf[1][1], s1);
    const float M = fmaxf(fmaxf(mm[0], mm[1]), fmaxf(mm[2], mm[3]));
    float L = 0.f, p0 = 0.f, p1 = 0.f;
#pragma unroll
    for (int s = 0; s < 4; ++s) {
      const float w = __builtin_amdgcn_exp2f(mm[s] - M);
      L = fmaf(w, ll[s], L); p0 = fmaf(w, s0[s], p0); p1 = fmaf(w, s1[s], p1);
    }
    const float inv = __builtin_amdgcn_rcpf(L);
    const v2u da = pack24(p0 * inv, y0[0], tagA), db = pack24(p1 * inv, y1[0], tagB);
    const u32 so = lane < 16 ? W_S + (u32)(((G * 8 + rbl) * 256 + cb * 64 + wave * 16 + lane) * 16) : OOB;
    __builtin_amdgcn_raw_buffer_store_b128(v4u{da.x, da.y, db.x, db.y}, wsr, so, 0, 16);
  }
  STAMP(10);
  if (b < 64 && wave == 0) {
    if (DLY3) __builtin_amdgcn_s_sleep(DLY3);
    const u32 o0 = W_S + (u32)(((lane >> 2) * 256 + U) * 16);
    u32 off[4] = {o0, o0 + 65536, o0 + 131072, o0 + 196608};
    v4u u[4];
    gather<4>(wsr, off, u, tagA, tagB);
    float s0 = 0.f, s1 = 0.f, q0 = 0.f, q1 = 0.f;
#pragma unroll
    for (int i = 0; i < 4; ++i) { s0 += un24a(u[i].x); q0 += un24b(u[i].x, u[i].y); s1 += un24a(u[i].z); q1 += un24b(u[i].z, u[i].w); }
    float s = sum16(tsum32(s0, s1)), y = sum16(tsum32(q0, q1));
    s += dppf<ROR8>(s); y += dppf<ROR8>(y);
    s += dppf<ROR4>(s); y += dppf<ROR4>(y);
    y = rbf(rbf(y) + bflo(bc));
    const float gt = rbf(y * __builtin_amdgcn_rcpf(1.0f + __builtin_amdgcn_exp2f(-y * LOG2E)));
    const float r = bflo(xc) + rbf(rbf(s) * gt);
    if ((lane & 28) == 0) out[col] = (u16)pkbf(r, r);
    if (b == 0 && lane == 0) __hip_atomic_store((GAS u32*)(ws + W_E), (tagB << 16) | tagA, __ATOMIC_RELAXED, __HIP_MEMORY_SCOPE_AGENT);
    STAMP(11);
  }
  STAMP_OUT();
}

extern "C" void kernel_launch(void* const* d_in, const int* in_sizes, int n_in, void* d_out, int out_size, void* d_ws, size_t ws_size,
                              hipStream_t stream) {
  const size_t win = (size_t)(32768 - 4096) * 128 * 2;
  hipLaunchKernelGGL(fused_decode, dim3(256), dim3(320), 0, stream, (const char*)d_in[0], (char*)d_ws, (const char*)d_in[3], (const char*)d_in[6],
                     (const char*)d_in[1] + win, (const char*)d_in[2] + win, (const char*)d_in[4], (const char*)d_in[8], (const char*)d_in[7],
                     (const char*)d_in[9], (u16*)d_out);
}
```

```cpp
#define DI 0
#define DJ 0
#include <hip/hip_runtime.h>
#include <hip/hip_bf16.h>
#include <stdint.h>

typedef unsigned u32;
typedef unsigned short u16;
typedef unsigned long long u64;
typedef u32 v4u __attribute__((ext_vector_type(4)));
typedef u32 v2u __attribute__((ext_vector_type(2)));
typedef float v4f __attribute__((ext_vector_type(4)));
typedef float v2f __attribute__((ext_vector_type(2)));
typedef short v4s __attribute__((ext_vector_type(4)));
typedef short v8s __attribute__((ext_vector_type(8)));
typedef __bf16 v8bf __attribute__((ext_vector_type(8)));
typedef __bf16 v2bf __attribute__((ext_vector_type(2)));
#define LAS __attribute__((address_space(3)))
#define GAS __attribute__((address_space(1)))
#define DEVI __device__ __forceinline__

#define Q_WAIT 4
#define DLY2 8
#define DLY3 8
#define STAMPS 0

constexpr int L_V = 0, L_WO = 32768, L_W2 = 49152, L_WQ = 57344, L_W1 = 73728, L_XN = 81920, L_QF = 83968;
constexpr int L_P = 84992, P_STR = 288, L_ML = L_P + 4 * P_STR, L_OP = L_ML + 128, L_ML2 = L_OP + 512, L_H = L_ML2 + 32, L_FLAG = L_H + 64;
constexpr int LDS_BYTES = L_FLAG + 16;
constexpr u32 W_E = 0, W_Q = 4096, W_H = W_Q + 32768, W_O = W_H + 32768, W_S = W_O + 524288, W_END = W_S + 262144;
constexpr u32 W_ST = 1u << 21;
constexpr u32 OOB = 0x7fffffffu;
constexpr int SPIN_CAP = 1 << 16;
constexpr float LOG2E = 1.4426950408889634f;

DEVI float asf(u32 x) { return __builtin_bit_cast(float, x); }
DEVI u32 asu(float x) { return __builtin_bit_cast(u32, x); }
DEVI u32 pkbf(float lo, float hi) { v2f p = {lo, hi}; return __builtin_bit_cast(u32, __builtin_convertvector(p, v2bf)); }
DEVI float rbf(float x) { return asf(pkbf(x, x) << 16); }
DEVI float bflo(u32 w) { return asf(w << 16); }
DEVI float bfhi(u32 w) { return asf(w & 0xffff0000u); }
template <int CTRL> DEVI u32 dppu(u32 x) { return (u32)__builtin_amdgcn_mov_dpp((int)x, CTRL, 0xf, 0xf, true); }
template <int CTRL> DEVI float dppf(float x) { return asf(dppu<CTRL>(asu(x))); }
constexpr int XOR1 = 0xB1, XOR2 = 0x4E, ROT1 = 0x39, ROT2 = 0x4E, ROT3 = 0x93, HMIR = 0x141, ROR4 = 0x124, ROR8 = 0x128;
DEVI float sum16(float x) { auto s = __builtin_amdgcn_permlane16_swap(asu(x), asu(x), false, false); return asf(s[0]) + asf(s[1]); }
DEVI float sum32(float x) { auto s = __builtin_amdgcn_permlane32_swap(asu(x), asu(x), false, false); return asf(s[0]) + asf(s[1]); }
DEVI float max16(float x) { auto s = __builtin_amdgcn_permlane16_swap(asu(x), asu(x), false, false); return fmaxf(asf(s[0]), asf(s[1])); }
DEVI float max32(float x) { auto s = __builtin_amdgcn_permlane32_swap(asu(x), asu(x), false, false); return fmaxf(asf(s[0]), asf(s[1])); }
DEVI float tsum16(float a, float b) { auto s = __builtin_amdgcn_permlane16_swap(asu(a), asu(b), false, false); return asf(s[0]) + asf(s[1]); }
DEVI float tsum32(float a, float b) { auto s = __builtin_amdgcn_permlane32_swap(asu(a), asu(b), false, false); return asf(s[0]) + asf(s[1]); }
DEVI float wave_sum(float x) { x += dppf<XOR1>(x); x += dppf<XOR2>(x); x += dppf<HMIR>(x); x += dppf<ROR8>(x); return sum32(sum16(x)); }
DEVI v4f mfma(v4u a, v4u b, v4f c) { return __builtin_amdgcn_mfma_f32_16x16x32_bf16(__builtin_bit_cast(v8bf, a), __builtin_bit_cast(v8bf, b), c, 0, 0, 0); }
DEVI v4u trfrag(LAS char* p0, LAS char* p1) {
  v4s a = __builtin_amdgcn_ds_read_tr16_b64_v4i16((LAS v4s*)p0), b = __builtin_amdgcn_ds_read_tr16_b64_v4i16((LAS v4s*)p1);
  return __builtin_bit_cast(v4u, __builtin_shufflevector(a, b, 0, 1, 2, 3, 4, 5, 6, 7));
}
DEVI v2u pack24(float a, float b, u32 tag) { return v2u{__builtin_amdgcn_perm(asu(a), asu(b), 0x07060501u), __builtin_amdgcn_perm(asu(b), tag, 0x07060100u)}; }
DEVI float un24a(u32 d0) { return asf(d0 & 0xffffff00u); }
DEVI float un24b(u32 d0, u32 d1) { return asf(__builtin_amdgcn_perm(d1, d0, 0x0706000cu)); }

#define BAR() asm volatile("s_waitcnt lgkmcnt(0)\n\ts_barrier" ::: "memory")
#define DMA4(sb, m0v, v0, v1, v2, v3)                                                                                   \
  asm volatile("s_mov_b32 m0, %1\n\ts_nop 0\n\tglobal_load_lds_dwordx4 %2, %0 nt\n\tglobal_load_lds_dwordx4 %3, %0 offset:1024 nt\n\t" \
               "global_load_lds_dwordx4 %4, %0 offset:2048 nt\n\tglobal_load_lds_dwordx4 %5, %0 offset:3072 nt"           \
               :: "s"(sb), "s"(m0v), "v"(v0), "v"(v1), "v"(v2), "v"(v3) : "memory")
#define DMA3(sb, m0v, v0, v1, v2)                                                                                       \
  asm volatile("s_mov_b32 m0, %1\n\ts_nop 0\n\tglobal_load_lds_dwordx4 %2, %0 nt\n\tglobal_load_lds_dwordx4 %3, %0 offset:1024 nt\n\t" \
               "global_load_lds_dwordx4 %4, %0 offset:2048 nt" :: "s"(sb), "s"(m0v), "v"(v0), "v"(v1), "v"(v2) : "memory")

#undef STAMPS
#define STAMPS 1
#define STAMP(i) do { asm volatile("s_memrealtime %0\n\ts_waitcnt lgkmcnt(0)" : "=s"(stv[i]) :: "memory"); } while (0)
#define STAMP_OUT() do { if (lane == 0) { for (int i_ = 0; i_ < 12; ++i_) ((GAS u64*)(ws + W_ST))[(b * 5 + wave) * 16 + i_] = stv[i_]; } } while (0)
#undef STAMP_OUT
#define STAMP_OUT() do { if (b == 0 && wave == 0) { u64 tt; const u64 until = stv[11] + 4 * (stv[DJ] - stv[DI]); do { asm volatile("s_memrealtime %0\n\ts_waitcnt lgkmcnt(0)" : "=s"(tt) :: "memory"); } while (tt < until); } } while (0)

template <int N>
DEVI void gather(__amdgpu_buffer_rsrc_t rs, u32 (&off)[N], v4u (&v)[N], u32 tagA, u32 tagB) {
#pragma unroll
  for (int i = 0; i < N; ++i) v[i] = v4u{0u, 0u, 0u, 0u};
  for (int it = 0; it < SPIN_CAP; ++it) {
    asm volatile("" ::: "memory");
    v4u n[N];
#pragma unroll
    for (int i = 0; i < N; ++i) n[i] = __builtin_amdgcn_raw_buffer_load_b128(rs, off[i], 0, 16);
    bool ok = true;
#pragma unroll
    for (int i = 0; i < N; ++i) {
      const bool good = ((((n[i].y ^ tagA) | (n[i].w ^ tagB)) & 0xffffu) == 0u) && off[i] != OOB;
      if (good) { v[i] = n[i]; off[i] = OOB; }
      ok = ok && off[i] == OOB;
    }
    if (__all(ok)) break;
  }
}

__global__ void __launch_bounds__(320)
fused_decode(const char* __restrict__ x, char* ws, const char* __restrict__ wq, const char* __restrict__ w1, const char* __restrict__ kw,
             const char* __restrict__ vw, const char* __restrict__ wo, const char* __restrict__ w2, const char* __restrict__ b1,
             const char* __restrict__ b2, u16* __restrict__ out) {
  __shared__ __attribute__((aligned(16))) char smem[LDS_BYTES];
  LAS char* sm = (LAS char*)smem;
  const u32 smb = (u32)(uintptr_t)sm;
  const int b = blockIdx.x, G = b & 7, j = b >> 3;
  const int lane = threadIdx.x & 63, wave = __builtin_amdgcn_readfirstlane(threadIdx.x >> 6);
  u64 stv[12];
  for (int i = 0; i < 12; ++i) stv[i] = 0;
  STAMP(0);
  v4u xr; u32 ev;
  asm volatile("global_load_dwordx4 %0, %2, %3\n\tglobal_load_dword %1, %4, %5 sc1"
               : "=&v"(xr), "=&v"(ev) : "v"(lane * 16), "s"(x), "v"(0), "s"(ws) : "memory");
  const bool proj = wave == 0 || (wave == 1 && j < 28);
  if (proj) {
    const u32 rs = wave == 0 ? 8192u : 3584u;
    const char* base = wave == 0 ? wq + (size_t)((j >> 4) * 256) * 8192 + G * 1024 + (j & 15) * 64 : w1 + (size_t)((j & 3) * 128) * 3584 + G * 448 + (j >> 2) * 64;
    const u32 v0 = (lane >> 2) * rs + (lane & 3) * 16, st = 16 * rs - 1024;
    const u32 ld = smb + (wave == 0 ? L_WQ : L_W1);
    DMA4(base, ld, v0, v0 + st, v0 + 2 * st, v0 + 3 * st);
    DMA4(base + 64 * rs, ld + 4096, v0, v0 + st, v0 + 2 * st, v0 + 3 * st);
    if (wave == 0) {
      DMA4(base + 128 * 8192, ld + 8192, v0, v0 + st, v0 + 2 * st, v0 + 3 * st);
      DMA4(base + 192 * 8192, ld + 12288, v0, v0 + st, v0 + 2 * st, v0 + 3 * st);
    }
  } else if (wave == 2) {
    if (lane == 0) *(LAS u32*)(sm + L_FLAG) = 0u;
  } else if (wave == 3) {
    *(LAS v4u*)(sm + L_W2 + 7168 + lane * 16) = v4u{0u, 0u, 0u, 0u};
  }
  BAR();
  const __amdgpu_buffer_rsrc_t wsr = __builtin_amdgcn_make_buffer_rsrc(ws, 0, W_END, 0x00020000);
  const int g4 = lane >> 4, c16 = lane & 15;
  const int rbl = j >> 2, cb = j & 3;
  if (wave == 4) {
    const u32 sw = 16 * (c16 ^ (g4 << 2));
    asm volatile("s_barrier" ::: "memory");
    {
      const char* base = vw + ((size_t)G * 32768 + j * 128) * 256;
      const u32 v0 = g4 * 256 + sw;
#pragma unroll
      for (int a = 0; a < 8; ++a) DMA4(base + a * 4096, smb + L_V + a * 4096, v0, v0 ^ 16, v0 ^ 32, v0 ^ 48);
    }
    STAMP(1);
    {
      const char* base = wo + (size_t)(G * 512 + rbl * 64) * 1024 + cb * 256;
      const u32 v0 = g4 * 1024 + sw;
#pragma unroll
      for (int a = 0; a < 4; ++a) DMA4(base + a * 16384, smb + L_WO + a * 4096, v0, (v0 ^ 16) + 3072, (v0 ^ 32) + 6144, (v0 ^ 48) + 9216);
    }
    const int hid0 = G * 224 + (rbl & 3) * 8 + (rbl >> 2) * 2;
    {
      const char* base = w2 + (size_t)hid0 * 1024 + cb * 256;
      const u32 v0 = ((g4 & 1) + 4 * (g4 >> 1)) * 1024 + sw;
      DMA4(base, smb + L_W2, v0, (v0 ^ 16) + 31744, (v0 ^ 32) + 2 * 31744, (v0 ^ 48) + 3 * 31744);
      DMA3(base + 4 * 32768, smb + L_W2 + 4096, v0, (v0 ^ 16) + 31744, (v0 ^ 32) + 2 * 31744);
    }
    STAMP(2);
    asm volatile("s_waitcnt vmcnt(23)" : "+v"(ev) :: "memory");
    STAMP(3);
    asm volatile("s_barrier" ::: "memory");
    const u32 e = __builtin_amdgcn_readfirstlane(ev), tagA = (e + 1u) & 0xffffu, tagB = ((e >> 16) + 1u) & 0xffffu;
    const int cbk = lane & 7;
    const bool hl = lane < 32 && cbk < 7;
    const int ck = cbk < 7 ? cbk : 6;
    const u32 ba = *(const GAS u32*)(b1 + (hid0 + 32 * ck) * 2), bb = *(const GAS u32*)(b1 + (hid0 + 32 * ck + 4) * 2);
    u32 off[1] = {hl ? W_H + (u32)(((G * 4 + (lane >> 3)) * 8 + cbk) * 128 + rbl * 16) : OOB};
    v4u u[1];
    gather<1>(wsr, off, u, tagA, tagB);
    float h0 = un24a(u[0].x), h4 = un24b(u[0].x, u[0].y), h1 = un24a(u[0].z), h5 = un24b(u[0].z, u[0].w);
    h0 += dppf<ROR8>(h0); h1 += dppf<ROR8>(h1); h4 += dppf<ROR8>(h4); h5 += dppf<ROR8>(h5);
    float hv[4] = {sum16(h0), sum16(h1), sum16(h4), sum16(h5)};
    const float bv[4] = {bflo(ba), bfhi(ba), bflo(bb), bfhi(bb)};
#pragma unroll
    for (int k = 0; k < 4; ++k) {
      const float t = rbf(rbf(hv[k]) + bv[k]);
      const float z = t * (1.5957691216057308f * LOG2E) * fmaf(0.044715f * t, t, 1.0f);
      hv[k] = t * __builtin_amdgcn_rcpf(1.0f + __builtin_amdgcn_exp2f(-z));
    }
    v2u hp = {pkbf(hv[0], hv[1]), pkbf(hv[2], hv[3])};
    if (cbk == 7) hp = v2u{0u, 0u};
    if (lane < 8) *(LAS v2u*)(sm + L_H + lane * 8) = hp;
    asm volatile("s_waitcnt vmcnt(0)" ::: "memory");
    STAMP(4);
    BAR();
    STAMP_OUT();
    return;
  }
  v4u kf[8];
  {
    const char* kb = kw + ((size_t)G * 32768 + j * 128 + wave * 32) * 256 + (((lane >> 2) & 3) * 2048 + g4 * 64 + (lane & 3) * 16);
#pragma unroll
    for (int t = 0; t < 8; ++t) kf[t] = __builtin_nontemporal_load((const GAS v4u*)(kb + t * 256));
  }
  asm volatile("s_barrier" ::: "memory");
  STAMP(1);
  if (wave == 0) asm volatile("s_waitcnt vmcnt(24)" ::: "memory");
  else if (proj) asm volatile("s_waitcnt vmcnt(16)" ::: "memory");
  else asm volatile("s_waitcnt vmcnt(8)" ::: "memory");
  asm volatile("" : "+v"(xr), "+v"(ev));
  const u32 e = __builtin_amdgcn_readfirstlane(ev), tagA = (e + 1u) & 0xffffu, tagB = ((e >> 16) + 1u) & 0xffffu;
  const u32 tagL = (lane & 1) ? tagB : tagA;
  if (proj) {
    float xv[8];
#pragma unroll
    for (int k = 0; k < 4; ++k) { xv[2 * k] = bflo(xr[k]); xv[2 * k + 1] = bfhi(xr[k]); }
    const float mean = wave_sum(((xv[0] + xv[1]) + (xv[2] + xv[3])) + ((xv[4] + xv[5]) + (xv[6] + xv[7]))) * (1.0f / 512.0f);
    float sq = 0.f;
#pragma unroll
    for (int k = 0; k < 8; ++k) { const float d = xv[k] - mean; sq = fmaf(d, d, sq); }
    const float var = wave_sum(sq) * (1.0f / 512.0f);
    const float mb = rbf(mean), den = rbf(__builtin_sqrtf(rbf(rbf(var) + 1e-5f)));
    v4u xn;
#pragma unroll
    for (int k = 0; k < 4; ++k) xn[k] = pkbf(rbf(xv[2 * k] - mb) / den, rbf(xv[2 * k + 1] - mb) / den);
    LAS char* xs = sm + L_XN + wave * 1024;
    *(LAS v4u*)(xs + lane * 16) = xn;
    const int nrep = wave == 0 ? 2 : 1;
    LAS char* ap = xs + (wave == 0 ? (j >> 4) * 512 : (j & 3) * 256) + g4 * 16;
    LAS char* bp = sm + (wave == 0 ? L_WQ : L_W1) + (8 * g4 + (c16 >> 2)) * 64 + (lane & 3) * 16;
    STAMP(2);
    asm volatile("s_waitcnt vmcnt(8)" ::: "memory");
    STAMP(3);
    v4f a0 = {0.f, 0.f, 0.f, 0.f}, a1 = a0;
    for (int r = 0; r < nrep; ++r) {
#pragma unroll
      for (int ks = 0; ks < 4; ++ks) {
        const v4u af = *(LAS v4u*)(ap + ks * 64);
        a0 = mfma(af, trfrag(bp + ks * 2048, bp + ks * 2048 + 256), a0);
        a1 = mfma(af, trfrag(bp + ks * 2048 + 8, bp + ks * 2048 + 264), a1);
      }
      ap += 256; bp += 8192;
    }
    const v2u d = pack24(a0[0], a1[0], tagL);
    const v4u unit = {d.x, d.y, dppu<XOR1>(d.x), dppu<XOR1>(d.y)};
    const u32 dst = wave == 0 ? W_Q + (u32)(G * 32 + j) * 128 : W_H + (u32)((G * 4 + (j & 3)) * 8 + (j >> 2)) * 128;
    const u32 so = (lane < 16 && !(lane & 1)) ? dst + ((lane >> 1) & 1) * 64 + (lane >> 2) * 16 : OOB;
    __builtin_amdgcn_raw_buffer_store_b128(unit, wsr, so, 0, 16);
    STAMP(4);
  }
  v4u qf;
  if (wave == 2) {
    asm volatile("s_waitcnt vmcnt(%0)" :: "i"(Q_WAIT) : "memory");
    STAMP(2);
    const u32 o0 = W_Q + (u32)(G * 32 + (c16 & 12) + g4) * 128 + (lane & 3) * 16;
    u32 off[4] = {o0, o0 + 64, o0 + 2048, o0 + 2048 + 64};
    v4u u[4];
    gather<4>(wsr, off, u, tagA, tagB);
    qf.x = pkbf(un24a(u[0].x) + un24a(u[2].x), un24a(u[0].z) + un24a(u[2].z));
    qf.y = pkbf(un24a(u[1].x) + un24a(u[3].x), un24a(u[1].z) + un24a(u[3].z));
    qf.z = pkbf(un24b(u[0].x, u[0].y) + un24b(u[2].x, u[2].y), un24b(u[0].z, u[0].w) + un24b(u[2].z, u[2].w));
    qf.w = pkbf(un24b(u[1].x, u[1].y) + un24b(u[3].x, u[3].y), un24b(u[1].z, u[1].w) + un24b(u[3].z, u[3].w));
    *(LAS v4u*)(sm + L_QF + lane * 16) = qf;
    asm volatile("s_waitcnt lgkmcnt(0)" ::: "memory");
    if (lane == 0) *(volatile LAS u32*)(sm + L_FLAG) = 1u;
    STAMP(3);
  } else {
    for (int it = 0; it < (SPIN_CAP << 6); ++it) {
      if (*(volatile LAS u32*)(sm + L_FLAG) != 0u) break;
      __builtin_amdgcn_s_sleep(1);
    }
    asm volatile("" ::: "memory");
    qf = *(LAS v4u*)(sm + L_QF + lane * 16);
  }
  STAMP(5);
  {
    v4f d[8];
#pragma unroll
    for (int t = 0; t < 8; ++t) d[t] = mfma(kf[t], qf, v4f{0.f, 0.f, 0.f, 0.f});
    const int u4 = lane & 3;
    float sc[2];
#pragma unroll
    for (int T = 0; T < 2; ++T) {
      float z[4];
#pragma unroll
      for (int r = 0; r < 4; ++r) {
        const float e0 = d[2 * ((0 - r) & 3) + T][0], e1 = d[2 * ((1 - r) & 3) + T][1], e2 = d[2 * ((2 - r) & 3) + T][2], e3 = d[2 * ((3 - r) & 3) + T][3];
        z[r] = u4 == 0 ? e0 : u4 == 1 ? e1 : u4 == 2 ? e2 : e3;
      }
      const float raw = (z[0] + dppf<ROT1>(z[1])) + (dppf<ROT2>(z[2]) + dppf<ROT3>(z[3]));
      sc[T] = rbf(rbf(raw) * 0.08838834764831845f);
    }
    float m = fmaxf(sc[0], sc[1]);
    m = fmaxf(m, dppf<XOR1>(m)); m = fmaxf(m, dppf<XOR2>(m)); m = max32(max16(m));
    const u32 pp = pkbf(__builtin_amdgcn_exp2f((sc[0] - m) * LOG2E), __builtin_amdgcn_exp2f((sc[1] - m) * LOG2E));
    const int hh = (lane >> 2) & 3;
    *(LAS u32*)(sm + L_P + hh * P_STR + (wave * 16 + g4 * 4 + u4) * 4) = pp;
    float l = bflo(pp) + bfhi(pp);
    l += dppf<XOR1>(l); l += dppf<XOR2>(l); l = sum32(sum16(l));
    if (lane < 16 && u4 < 2) *(LAS float*)(sm + L_ML + hh * 32 + u4 * 16 + wave * 4) = u4 ? l : m;
  }
  STAMP(6);
  BAR();
  STAMP(7);
  const u32 trsw = ((c16 >> 2) << 2);
  LAS char* tb[2][2];
#pragma unroll
  for (int bk = 0; bk < 2; ++bk)
#pragma unroll
    for (int t = 0; t < 2; ++t)
      tb[bk][t] = sm + (8 * g4 + 4 * t + (c16 >> 2)) * 256 + 16 * ((2 * (2 * wave + bk) + ((lane >> 1) & 1)) ^ (trsw | ((2 * g4 + t) & 3))) + 8 * (lane & 1);
  {
    const v4f mm = *(LAS v4f*)(sm + L_ML + g4 * 32), ll = *(LAS v4f*)(sm + L_ML + g4 * 32 + 16);
    v4f d0[4], d1[4];
#pragma unroll
    for (int s = 0; s < 4; ++s) {
      const v4u af = *(LAS v4u*)(sm + L_P + (c16 >> 2) * P_STR + s * 64 + g4 * 16);
      d0[s] = mfma(af, trfrag(tb[0][0] + L_V + s * 8192, tb[0][1] + L_V + s * 8192), v4f{0.f, 0.f, 0.f, 0.f});
      d1[s] = mfma(af, trfrag(tb[1][0] + L_V + s * 8192, tb[1][1] + L_V + s * 8192), v4f{0.f, 0.f, 0.f, 0.f});
    }
    const float M = fmaxf(fmaxf(mm[0], mm[1]), fmaxf(mm[2], mm[3]));
    float L = 0.f, o0 = 0.f, o1 = 0.f;
#pragma unroll
    for (int s = 0; s < 4; ++s) {
      const float w = __builtin_amdgcn_exp2f((mm[s] - M) * LOG2E);
      L = fmaf(w, ll[s], L); o0 = fmaf(w, d0[s][0], o0); o1 = fmaf(w, d1[s][0], o1);
    }
    const float inv = __builtin_amdgcn_rcpf(L);
    const u32 lse = asu(fmaf(M, LOG2E, __builtin_amdgcn_logf(L)));
    const u32 da = pkbf(o0 * inv, o1 * inv), db = ((lane & 1) ? (lse << 16) : (lse & 0xffff0000u)) | tagL;
    const v4u unit = {da, db, dppu<XOR1>(da), dppu<XOR1>(db)};
    const u32 so = (lane & 1) ? OOB : W_O + (u32)((((G * 4 + g4) * 4 + wave) * 32 + j) * 128 + (c16 >> 1) * 16);
    __builtin_amdgcn_raw_buffer_store_b128(unit, wsr, so, 0, 16);
  }
  STAMP(8);
  const int f = ((b & 7) << 3) | (b >> 3);
  const int U = 4 * (f & 63) + (lane & 3), col = 32 * (U >> 4) + (U & 15) + 16 * (lane >> 5);
  u32 xc = 0, bc = 0;
  if (b < 64 && wave == 0) { xc = *(const GAS u16*)(x + col * 2); bc = *(const GAS u16*)(b2 + col * 2); }
  BAR();
  v4u wf[2][2]; v4f y0, y1;
  {
#pragma unroll
    for (int ks = 0; ks < 2; ++ks)
#pragma unroll
      for (int bk = 0; bk < 2; ++bk) wf[ks][bk] = trfrag(tb[bk][0] + L_WO + ks * 8192, tb[bk][1] + L_WO + ks * 8192);
    const v4u hf = *(LAS v4u*)(sm + L_H + g4 * 16);
    y0 = mfma(hf, trfrag(tb[0][0] + L_W2, tb[0][1] + L_W2), v4f{0.f, 0.f, 0.f, 0.f});
    y1 = mfma(hf, trfrag(tb[1][0] + L_W2, tb[1][1] + L_W2), v4f{0.f, 0.f, 0.f, 0.f});
  }
  {
    if (DLY2) __builtin_amdgcn_s_sleep(DLY2);
    const u32 o0 = W_O + (u32)(((G * 4 + (rbl >> 1)) * 4 + (rbl & 1) * 2) * 4096 + wave * 1024 + lane * 16);
    u32 off[2] = {o0, o0 + 4096};
    v4u u[2];
    gather<2>(wsr, off, u, tagA, tagB);
    STAMP(9);
    const float lse = asf(__builtin_amdgcn_perm(u[0].y, u[0].w, 0x07060302u));
    float M = fmaxf(lse, dppf<ROR8>(lse)); M = max32(max16(M));
    const float w = __builtin_amdgcn_exp2f(lse - M);
    float L = w + dppf<ROR8>(w); L = sum32(sum16(L));
    const float t0 = tsum32(w * bflo(u[0].x), w * bflo(u[1].x)), t1 = tsum32(w * bfhi(u[0].x), w * bfhi(u[1].x));
    const float t2 = tsum32(w * bflo(u[0].z), w * bflo(u[1].z)), t3 = tsum32(w * bfhi(u[0].z), w * bfhi(u[1].z));
    float r0 = tsum16(t0, t1), r1 = tsum16(t2, t3);
    r0 += dppf<ROR8>(r0); r1 += dppf<ROR8>(r1);
    *(LAS u32*)(sm + L_OP + wave * 128 + (g4 * 8 + (lane & 7)) * 4) = pkbf(r0, r1);
    if (lane < 2) *(LAS float*)(sm + L_ML2 + lane * 16 + wave * 4) = lane ? L : M;
  }
  BAR();
  {
    const v4f mm = *(LAS v4f*)(sm + L_ML2), ll = *(LAS v4f*)(sm + L_ML2 + 16);
    const v4u a0 = *(LAS v4u*)(sm + L_OP + (lane & 3) * 128 + g4 * 16), a1 = *(LAS v4u*)(sm + L_OP + (lane & 3) * 128 + 64 + g4 * 16);
    v4f s0 = mfma(a0, wf[0][0], v4f{0.f, 0.f, 0.f, 0.f}), s1 = mfma(a0, wf[0][1], v4f{0.f, 0.f, 0.f, 0.f});
    s0 = mfma(a1, wf[1][0], s0); s1 = mfma(a1, wf[1][1], s1);
    const float M = fmaxf(fmaxf(mm[0], mm[1]), fmaxf(mm[2], mm[3]));
    float L = 0.f, p0 = 0.f, p1 = 0.f;
#pragma unroll
    for (int s = 0; s < 4; ++s) {
      const float w = __builtin_amdgcn_exp2f(mm[s] - M);
      L = fmaf(w, ll[s], L); p0 = fmaf(w, s0[s], p0); p1 = fmaf(w, s1[s], p1);
    }
    const float inv = __builtin_amdgcn_rcpf(L);
    const v2u da = pack24(p0 * inv, y0[0], tagA), db = pack24(p1 * inv, y1[0], tagB);
    const u32 so = lane < 16 ? W_S + (u32)(((G * 8 + rbl) * 256 + cb * 64 + wave * 16 + lane) * 16) : OOB;
    __builtin_amdgcn_raw_buffer_store_b128(v4u{da.x, da.y, db.x, db.y}, wsr, so, 0, 16);
  }
  STAMP(10);
  if (b < 64 && wave == 0) {
    if (DLY3) __builtin_amdgcn_s_sleep(DLY3);
    const u32 o0 = W_S + (u32)(((lane >> 2) * 256 + U) * 16);
    u32 off[4] = {o0, o0 + 65536, o0 + 131072, o0 + 196608};
    v4u u[4];
    gather<4>(wsr, off, u, tagA, tagB);
    float s0 = 0.f, s1 = 0.f, q0 = 0.f, q1 = 0.f;
#pragma unroll
    for (int i = 0; i < 4; ++i) { s0 += un24a(u[i].x); q0 += un24b(u[i].x, u[i].y); s1 += un24a(u[i].z); q1 += un24b(u[i].z, u[i].w); }
    float s = sum16(tsum32(s0, s1)), y = sum16(tsum32(q0, q1));
    s += dppf<ROR8>(s); y += dppf<ROR8>(y);
    s += dppf<ROR4>(s); y += dppf<ROR4>(y);
    y = rbf(rbf(y) + bflo(bc));
    const float gt = rbf(y * __builtin_amdgcn_rcpf(1.0f + __builtin_amdgcn_exp2f(-y * LOG2E)));
    const float r = bflo(xc) + rbf(rbf(s) * gt);
    if ((lane & 28) == 0) out[col] = (u16)pkbf(r, r);
    if (b == 0 && lane == 0) __hip_atomic_store((GAS u32*)(ws + W_E), (tagB << 16) | tagA, __ATOMIC_RELAXED, __HIP_MEMORY_SCOPE_AGENT);
    STAMP(11);
  }
  STAMP_OUT();
}

extern "C" void kernel_launch(void* const* d_in, const int* in_sizes, int n_in, void* d_out, int out_size, void* d_ws, size_t ws_size,
                              hipStream_t stream) {
  const size_t win = (size_t)(32768 - 4096) * 128 * 2;
  hipLaunchKernelGGL(fused_decode, dim3(256), dim3(320), 0, stream, (const char*)d_in[0], (char*)d_ws, (const char*)d_in[3], (const char*)d_in[6],
                     (const char*)d_in[1] + win, (const char*)d_in[2] + win, (const char*)d_in[4], (const char*)d_in[8], (const char*)d_in[7],
                     (const char*)d_in[9], (u16*)d_out);
}
```

```cpp
#include <hip/hip_runtime.h>
#include <stdint.h>

__global__ void __launch_bounds__(448, 2) __attribute__((aligned(4096)))
fused_decode(const char* wq, const char* w1, const char* kw, const char* vw, const char* wo, const char* w2, const char* x, char* ws,
             const char* b1, const char* b2, char* out) {
  __shared__ __attribute__((aligned(16))) char smem[122880];
  unsigned lds_base = (unsigned)(uintptr_t)(__attribute__((address_space(3))) char*)smem;
  asm volatile(
      "START_%=:\n"
      "s_load_dwordx16 s[8:23], s[0:1], 0x0\n"
      "s_memrealtime s[6:7]\n"
      "s_mov_b32 s42, 1052672\n"
      "s_mov_b32 s43, 0x20000\n"
      "s_mov_b32 s52, 0x05040100\n"
      "s_mov_b32 s53, 0x07060501\n"
      "s_mov_b32 s54, 0x07060100\n"
      "s_mov_b32 s55, 0x0706000c\n"
      "s_mov_b32 s56, 0x07060302\n"
      "v_readfirstlane_b32 s30, v0\n"
      "v_and_b32 v1, 63, v0\n"
      "v_mov_b32 v3, 0\n"
      "s_and_b32 s31, s2, 7\n"
      "s_lshr_b32 s32, s2, 3\n"
      "s_lshr_b32 s33, s2, 5\n"
      "s_bfe_u32 s34, s2, 0x20003\n"
      "s_lshr_b32 s30, s30, 6\n"
      "v_lshlrev_b32 v2, 4, v1\n"
      "s_cmp_eq_u32 s30, 0\n"
      "s_cbranch_scc1 WEIGHTS_%=\n"
      "s_cmp_gt_u32 s30, 4\n"
      "s_cbranch_scc1 STREAM0_%=\n"
      "s_waitcnt lgkmcnt(0)\n"
      "s_memrealtime s[4:5]\n"
      "global_load_dwordx4 v[4:7], v2, s[20:21]\n"
      "s_load_dword s3, s[22:23], 0x0\n"
      "s_load_dwordx4 s[24:27], s[0:1], 0x48\n"
      "s_barrier\n"
      "s_branch COMPUTE_%=\n"
      "STREAM0_%=:\n"
      "s_cmp_eq_u32 s30, 5\n"
      "s_cbranch_scc1 STREAM_%=\n"
      "s_getpc_b64 s[44:45]\n"
      "v_lshlrev_b32 v21, 7, v1\n"
      "s_and_b32 s44, s44, 0xfffff000\n"
      "v_min_u32 v21, END_%=-START_%=, v21\n"
      "global_load_dword v60, v21, s[44:45]\n"
      "v_mov_b32 v4, 0\n"
      "v_mov_b32 v5, 0\n"
      "v_mov_b32 v6, 0\n"
      "v_mov_b32 v7, 0\n"
      "v_add_u32 v8, 121856, v2\n"
      "ds_write_b128 v3, v[4:7]\n"
      "ds_write_b128 v3, v[4:7] offset:16\n"
      "ds_write_b128 v8, v[4:7]\n"
      "s_waitcnt lgkmcnt(0)\n"
      "s_memrealtime s[4:5]\n"
      "s_cmp_lg_u32 s32, 0\n"
      "s_cbranch_scc1 NOW_%=\n"
      "s_lshl_b32 s44, s31, 3\n"
      "v_mov_b32 v6, s44\n"
      "s_mov_b64 exec, 1\n"
      "s_waitcnt lgkmcnt(0)\n"
      "v_mov_b32 v4, s4\n"
      "v_mov_b32 v5, s5\n"
      "global_store_dwordx2 v6, v[4:5], s[22:23] offset:2048 sc1\n"
      "s_mov_b64 exec, -1\n"
      "NOW_%=:\n"
      "s_load_dword s3, s[22:23], 0x0\n"
      "s_load_dwordx4 s[24:27], s[0:1], 0x40\n"
      "s_barrier\n"
      "s_branch TAILP_%=\n"
      "WEIGHTS_%=:\n"
      "v_lshrrev_b32 v12, 3, v1\n"
      "v_and_b32 v13, 7, v1\n"
      "v_and_b32 v8, 2, v12\n"
      "v_xor_b32 v9, v13, v8\n"
      "v_lshlrev_b32 v9, 4, v9\n"
      "v_lshl_or_b32 v4, v12, 13, v9\n"
      "v_xor_b32 v5, 64, v4\n"
      "v_add_u32 v6, 0x1f800, v4\n"
      "v_add_u32 v7, 0x2f400, v5\n"
      "v_add_u32 v5, 0xfc00, v5\n"
      "s_lshr_b32 s48, s32, 3\n"
      "s_lshl_b32 s44, s48, 20\n"
      "s_lshl_b32 s45, s31, 10\n"
      "s_or_b32 s44, s44, s45\n"
      "s_and_b32 s46, s32, 7\n"
      "s_lshl_b32 s45, s46, 7\n"
      "s_or_b32 s44, s44, s45\n"
      "s_mov_b32 m0, 8192\n"
      "s_waitcnt lgkmcnt(0)\n"
      "s_add_u32 s8, s8, s44\n"
      "s_addc_u32 s9, s9, 0\n"
      "global_load_lds_dwordx4 v4, s[8:9] nt\n"
      "global_load_lds_dwordx4 v5, s[8:9] offset:1024 nt\n"
      "global_load_lds_dwordx4 v6, s[8:9] offset:2048 nt\n"
      "global_load_lds_dwordx4 v7, s[8:9] offset:3072 nt\n"
      "s_memrealtime s[4:5]\n"
      "s_mov_b32 m0, 12288\n"
      "s_add_u32 s8, s8, 0x40000\n"
      "s_addc_u32 s9, s9, 0\n"
      "global_load_lds_dwordx4 v4, s[8:9] nt\n"
      "global_load_lds_dwordx4 v5, s[8:9] offset:1024 nt\n"
      "global_load_lds_dwordx4 v6, s[8:9] offset:2048 nt\n"
      "global_load_lds_dwordx4 v7, s[8:9] offset:3072 nt\n"
      "s_mov_b32 m0, 16384\n"
      "s_add_u32 s8, s8, 0x40000\n"
      "s_addc_u32 s9, s9, 0\n"
      "global_load_lds_dwordx4 v4, s[8:9] nt\n"
      "global_load_lds_dwordx4 v5, s[8:9] offset:1024 nt\n"
      "global_load_lds_dwordx4 v6, s[8:9] offset:2048 nt\n"
      "global_load_lds_dwordx4 v7, s[8:9] offset:3072 nt\n"
      "s_mov_b32 m0, 20480\n"
      "s_add_u32 s8, s8, 0x40000\n"
      "s_addc_u32 s9, s9, 0\n"
      "global_load_lds_dwordx4 v4, s[8:9] nt\n"
      "global_load_lds_dwordx4 v5, s[8:9] offset:1024 nt\n"
      "global_load_lds_dwordx4 v6, s[8:9] offset:2048 nt\n"
      "global_load_lds_dwordx4 v7, s[8:9] offset:3072 nt\n"
      "s_barrier\n"
      "s_load_dword s3, s[22:23], 0x0\n"
      "s_mov_b32 s40, s22\n"
      "s_and_b32 s41, s23, 0xffff\n"
      "s_lshl_b32 s44, s31, 14\n"
      "s_add_u32 s45, s44, 4096\n"
      "v_add_u32 v16, s45, v2\n"
      "s_movk_i32 s59, 0x1000\n"
      "s_movk_i32 s60, 0x2000\n"
      "s_movk_i32 s61, 0x3000\n"
      "v_lshlrev_b32 v18, 2, v1\n"
      "v_mov_b32 v61, 1\n"
      "v_mov_b32 v25, 1\n"
      "s_waitcnt vmcnt(0)\n"
      "ds_write_b32 v3, v25 offset:8\n"
      "s_branch POLLER_%=\n"
      "STREAM_%=:\n"
      "v_lshrrev_b32 v2, 4, v1\n"
      "v_and_b32 v4, 15, v1\n"
      "v_lshlrev_b32 v4, 4, v4\n"
      "v_lshlrev_b32 v12, 6, v2\n"
      "v_xor_b32 v4, v4, v12\n"
      "v_lshl_or_b32 v12, v2, 8, v4\n"
      "v_xor_b32 v13, 16, v12\n"
      "v_xor_b32 v14, 32, v12\n"
      "v_xor_b32 v15, 48, v12\n"
      "s_lshl_b32 s46, s31, 23\n"
      "s_waitcnt lgkmcnt(0)\n"
      "s_memrealtime s[4:5]\n"
      "s_barrier\n"
      "s_add_u32 s12, s12, s46\n"
      "s_addc_u32 s13, s13, 0\n"
      "s_add_u32 s14, s14, s46\n"
      "s_addc_u32 s15, s15, 0\n"
      "s_lshl_b32 s58, s32, 12\n"
      "s_and_b32 s58, s58, 126976\n"
      "s_lshl_b32 s57, s31, 17\n"
      "s_or_b32 s58, s58, s57\n"
      "s_mov_b32 s57, s58\n"
      "s_mov_b32 m0, 28672\n"
      "v_mov_b32 v25, 0\n"
      "s_mov_b32 s49, 0\n"
      "s_waitcnt lgkmcnt(0)\n"
      "s_sub_u32 s47, s4, s6\n"
      "s_cmp_gt_i32 s47, 48\n"
      "s_cselect_b32 s47, s32, 0\n"
      "s_lshl_b32 s44, s47, 15\n"
      "v_mov_b32 v7, s44\n"
      "s_lshl_b32 s44, s31, 5\n"
      "s_add_u32 s44, s44, s47\n"
      "s_lshl_b32 s45, s44, 14\n"
      "v_mov_b32 v5, s45\n"
      "s_mul_i32 s45, s44, 0x1c00\n"
      "v_mov_b32 v6, s45\n"
      "s_and_b32 s44, s44, 63\n"
      "s_mul_i32 s45, s44, 0x7000\n"
      "s_lshl_b32 s44, s31, 7\n"
      "s_add_u32 s45, s45, s44\n"
      "v_mov_b32 v8, s45\n"
      "s_mov_b64 exec, 1\n"
      "global_load_dword v60, v7, s[12:13] nt\n"
      "global_load_dword v60, v7, s[14:15] nt\n"
      "global_load_dword v60, v8, s[10:11] nt\n"
      "global_load_dword v60, v5, s[16:17] nt\n"
      "global_load_dword v60, v6, s[18:19] nt\n"
      "s_mov_b64 exec, -1\n"
      "T1_%=:\n"
      "s_memrealtime s[6:7]\n"
      "s_waitcnt lgkmcnt(0)\n"
      "s_sub_u32 s6, s6, s4\n"
      "s_cmp_lt_i32 s6, 64\n"
      "s_cbranch_scc1 T1_%=\n"
      "KV0_%=:\n"
      "s_add_u32 m0, m0, 0x1000\n"
      "s_add_u32 s60, s12, s58\n"
      "s_addc_u32 s61, s13, 0\n"
      "s_add_u32 s62, s58, 0x1000\n"
      "s_and_b32 s62, s62, 0x1f000\n"
      "s_add_u32 s58, s58, 0x20000\n"
      "s_and_b32 s58, s58, 0xe0000\n"
      "s_or_b32 s58, s58, s62\n"
      "global_load_lds_dwordx4 v12, s[60:61] nt\n"
      "global_load_lds_dwordx4 v13, s[60:61] offset:1024 nt\n"
      "global_load_lds_dwordx4 v14, s[60:61] offset:2048 nt\n"
      "global_load_lds_dwordx4 v15, s[60:61] offset:3072 nt\n"
      "v_add_u32 v25, 1, v25\n"
      "s_add_u32 s49, s49, 1\n"
      "s_cmp_eq_u32 s49, 8\n"
      "s_cmov_b64 s[12:13], s[14:15]\n"
      "s_cmov_b32 s58, s57\n"
      "s_cmp_lt_u32 s49, 16\n"
      "s_waitcnt vmcnt(24)\n"
      "ds_write_b32 v3, v25 offset:0\n"
      "s_cbranch_scc1 KV0_%=\n"
      "v_mov_b32 v25, 1\n"
      "s_mov_b64 exec, 1\n"
      "s_waitcnt vmcnt(0)\n"
      "ds_add_u32 v3, v25 offset:12\n"
      "s_waitcnt lgkmcnt(0)\n"
      "s_endpgm\n"
      "TAILP_%=:\n"
      "s_mov_b32 s40, s22\n"
      "s_and_b32 s41, s23, 0xffff\n"
      "v_lshrrev_b32 v12, 3, v1\n"
      "v_and_b32 v13, 7, v1\n"
      "v_min_u32 v10, 6, v13\n"
      "s_mul_i32 s44, s31, 0x1c0\n"
      "s_mul_i32 s45, s33, 56\n"
      "s_add_u32 s44, s44, s45\n"
      "v_lshl_add_u32 v11, v10, 3, s44\n"
      "v_lshrrev_b32 v14, 5, v1\n"
      "v_bfe_u32 v15, v1, 4, 1\n"
      "v_lshl_or_b32 v14, v15, 1, v14\n"
      "v_lshl_add_u32 v11, v14, 1, v11\n"
      "s_lshl_b32 s44, s31, 14\n"
      "v_lshlrev_b32 v17, 12, v12\n"
      "v_lshl_or_b32 v17, v10, 5, v17\n"
      "s_and_b32 s45, s31, 1\n"
      "s_lshl_b32 s45, s45, 3\n"
      "s_add_u32 s45, s45, s33\n"
      "s_mul_i32 s45, s45, 0xe0\n"
      "s_and_b32 s44, s44, 0x18000\n"
      "s_add_u32 s45, s45, s44\n"
      "s_add_u32 s45, s45, 135168\n"
      "v_add_u32 v17, s45, v17\n"
      "v_lshlrev_b32 v19, 3, v13\n"
      "v_lshl_add_u32 v19, v14, 1, v19\n"
      "s_waitcnt lgkmcnt(0)\n"
      "s_add_u32 s45, s3, 2\n"
      "s_and_b32 s45, s45, 0xfffe\n"
      "s_add_u32 s44, s3, 0x20000\n"
      "s_bitset1_b32 s44, 16\n"
      "s_lshr_b32 s37, s44, 16\n"
      "s_and_b32 s44, s44, 0xffff0000\n"
      "s_or_b32 s36, s44, s45\n"
      "v_lshrrev_b32 v4, 4, v1\n"
      "v_and_b32 v5, 15, v1\n"
      "v_lshlrev_b32 v5, 4, v5\n"
      "v_lshlrev_b32 v6, 6, v4\n"
      "v_xor_b32 v5, v5, v6\n"
      "v_lshl_or_b32 v28, v4, 10, v5\n"
      "v_xor_b32 v29, 16, v28\n"
      "v_xor_b32 v30, 32, v28\n"
      "v_xor_b32 v31, 48, v28\n"
      "v_add_u32 v29, 3072, v29\n"
      "v_add_u32 v30, 6144, v30\n"
      "v_add_u32 v31, 9216, v31\n"
      "s_lshl_b32 s49, s34, 8\n"
      "s_lshl_b32 s47, s31, 19\n"
      "s_lshl_b32 s48, s33, 16\n"
      "s_or_b32 s47, s47, s48\n"
      "s_or_b32 s47, s47, s49\n"
      "s_add_u32 s16, s16, s47\n"
      "s_addc_u32 s17, s17, 0\n"
      "s_lshl_b32 s58, s33, 14\n"
      "s_and_b32 s58, s58, 0xc000\n"
      "s_mul_i32 s48, s31, 0x38000\n"
      "s_add_u32 s48, s48, s49\n"
      "s_mul_i32 s49, s33, 0x7000\n"
      "s_add_u32 s48, s48, s49\n"
      "s_add_u32 s18, s18, s48\n"
      "s_addc_u32 s19, s19, 0\n"
      "v_lshrrev_b32 v5, 3, v1\n"
      "v_and_b32 v6, 7, v1\n"
      "v_and_b32 v7, 2, v5\n"
      "v_xor_b32 v6, v6, v7\n"
      "v_mul_u32_u24 v5, 0xe00, v5\n"
      "v_lshl_add_u32 v32, v6, 4, v5\n"
      "v_xor_b32 v33, 64, v32\n"
      "v_add_u32 v34, 0xd800, v32\n"
      "v_add_u32 v35, 0x14400, v33\n"
      "v_add_u32 v33, 0x6c00, v33\n"
      "s_and_b32 s46, s32, 7\n"
      "s_cmp_eq_u32 s46, 3\n"
      "s_cselect_b32 s47, 0, -1\n"
      "s_cmp_gt_u32 s46, 3\n"
      "s_subb_u32 s46, s46, 0\n"
      "s_lshl_b32 s46, s46, 7\n"
      "s_lshr_b32 s45, s31, 1\n"
      "s_mul_i32 s45, s45, 0x380\n"
      "s_add_u32 s46, s46, s45\n"
      "s_and_b32 s45, s31, 1\n"
      "s_lshl_b32 s45, s45, 2\n"
      "s_lshr_b32 s48, s32, 3\n"
      "s_add_u32 s45, s45, s48\n"
      "s_mul_i32 s45, s45, 0x38000\n"
      "s_add_u32 s45, s45, s46\n"
      "s_add_u32 s10, s10, s45\n"
      "s_addc_u32 s11, s11, 0\n"
      "v_and_b32 v32, s47, v32\n"
      "v_and_b32 v33, s47, v33\n"
      "v_and_b32 v34, s47, v34\n"
      "v_and_b32 v35, s47, v35\n"
      "s_and_b32 s45, s31, 1\n"
      "s_lshl_b32 s45, s45, 2\n"
      "s_lshr_b32 s46, s32, 3\n"
      "s_add_u32 s45, s45, s46\n"
      "s_lshl_b32 s46, s45, 7\n"
      "s_add_u32 s46, s46, 4096\n"
      "v_and_b32 v5, 48, v1\n"
      "v_add_u32 v36, s46, v5\n"
      "v_bfe_u32 v5, v1, 2, 2\n"
      "v_lshl_or_b32 v5, v4, 3, v5\n"
      "v_bfe_u32 v6, v1, 3, 2\n"
      "v_lshlrev_b32 v6, 5, v6\n"
      "v_and_b32 v7, 3, v1\n"
      "v_lshl_or_b32 v6, v7, 3, v6\n"
      "v_lshl_or_b32 v37, v5, 7, v6\n"
      "v_xor_b32 v40, 32, v37\n"
      "v_xor_b32 v41, 64, v37\n"
      "v_xor_b32 v42, 0x60, v37\n"
      "s_lshl_b32 s45, s45, 12\n"
      "s_lshl_b32 s47, s31, 14\n"
      "s_and_b32 s47, s47, 0x18000\n"
      "s_add_u32 s45, s45, s47\n"
      "s_and_b32 s46, s32, 7\n"
      "s_cmp_eq_u32 s46, 3\n"
      "s_cselect_b32 s46, 8, s46\n"
      "s_cmp_gt_u32 s46, 3\n"
      "s_subb_u32 s46, s46, 0\n"
      "s_cmp_eq_u32 s46, 7\n"
      "s_cselect_b32 s47, 0x40000000, 0\n"
      "s_lshl_b32 s46, s46, 9\n"
      "s_add_u32 s45, s45, s46\n"
      "s_add_u32 s45, s45, s47\n"
      "s_add_u32 s45, s45, 135168\n"
      "v_lshl_add_u32 v38, v1, 3, s45\n"
      "v_lshlrev_b32 v20, 3, v13\n"
      "v_mov_b32 v62, 200\n"
      "T2_%=:\n"
      "s_memrealtime s[6:7]\n"
      "s_waitcnt lgkmcnt(0)\n"
      "s_sub_u32 s6, s6, s4\n"
      "s_cmp_lt_i32 s6, 56\n"
      "s_cbranch_scc1 T2_%=\n"
      "global_load_dwordx2 v[22:23], v20, s[22:23] offset:2048 sc1\n"
      "T3_%=:\n"
      "s_memrealtime s[6:7]\n"
      "s_waitcnt lgkmcnt(0)\n"
      "s_sub_u32 s6, s6, s4\n"
      "s_cmp_lt_i32 s6, 100\n"
      "s_cbranch_scc1 T3_%=\n"
      "global_load_dwordx2 v[24:25], v20, s[22:23] offset:2048 sc1\n"
      "T4_%=:\n"
      "s_memrealtime s[6:7]\n"
      "s_waitcnt lgkmcnt(0)\n"
      "s_sub_u32 s6, s6, s4\n"
      "s_cmp_lt_i32 s6, 140\n"
      "s_cbranch_scc1 T4_%=\n"
      "global_load_dwordx2 v[26:27], v20, s[22:23] offset:2048 sc1\n"
      "s_waitcnt vmcnt(2)\n"
      "v_subrev_u32 v22, s4, v22\n"
      "v_cmp_ne_u32 s[44:45], s5, v23\n"
      "v_add_u32 v21, v22, v62\n"
      "v_cmp_lt_u32 vcc, 0x190, v21\n"
      "s_or_b64 vcc, vcc, s[44:45]\n"
      "s_cbranch_scc0 RD_%=\n"
      "s_waitcnt vmcnt(1)\n"
      "v_subrev_u32 v22, s4, v24\n"
      "v_cmp_ne_u32 s[44:45], s5, v25\n"
      "v_add_u32 v21, v22, v62\n"
      "v_cmp_lt_u32 vcc, 0x190, v21\n"
      "s_or_b64 vcc, vcc, s[44:45]\n"
      "s_cbranch_scc0 RD_%=\n"
      "s_waitcnt vmcnt(0)\n"
      "v_subrev_u32 v22, s4, v26\n"
      "v_cmp_ne_u32 s[44:45], s5, v27\n"
      "v_add_u32 v21, v22, v62\n"
      "v_cmp_lt_u32 vcc, 0x190, v21\n"
      "s_or_b64 vcc, vcc, s[44:45]\n"
      "RD_%=:\n"
      "s_nop 0\n"
      "v_cndmask_b32 v22, v22, v3, vcc\n"
      "s_nop 1\n"
      "v_add_u32_dpp v23, v22, v22 quad_perm:[1,0,3,2] row_mask:0xf bank_mask:0xf bound_ctrl:1\n"
      "v_max_i32_dpp v22, v22, v22 quad_perm:[1,0,3,2] row_mask:0xf bank_mask:0xf bound_ctrl:1\n"
      "s_nop 0\n"
      "v_add_u32_dpp v23, v23, v23 quad_perm:[2,3,0,1] row_mask:0xf bank_mask:0xf bound_ctrl:1\n"
      "v_max_i32_dpp v22, v22, v22 quad_perm:[2,3,0,1] row_mask:0xf bank_mask:0xf bound_ctrl:1\n"
      "s_nop 0\n"
      "v_add_u32_dpp v23, v23, v23 row_half_mirror row_mask:0xf bank_mask:0xf bound_ctrl:1\n"
      "v_max_i32_dpp v22, v22, v22 row_half_mirror row_mask:0xf bank_mask:0xf bound_ctrl:1\n"
      "v_readfirstlane_b32 s45, v23\n"
      "v_readfirstlane_b32 s44, v22\n"
      "s_add_u32 s29, s4, s44\n"
      "s_ashr_i32 s45, s45, 3\n"
      "s_sub_u32 s45, s44, s45\n"
      "s_sub_u32 s45, s45, 16\n"
      "s_max_i32 s45, s45, 0\n"
      "s_min_i32 s45, s45, 9\n"
      "s_mul_i32 s45, s45, -10\n"
      "s_ashr_i32 s45, s45, 2\n"
      "s_sub_u32 s44, s44, s45\n"
      "s_add_u32 s4, s4, s44\n"
      "s_add_u32 s44, s44, 0x400\n"
      "v_mov_b32 v22, s44\n"
      "ds_write_b32 v3, v22 offset:20\n"
      "s_branch PAD1_%=\n"
      "COMPUTE_%=:\n"
      "s_sub_u32 s35, s30, 1\n"
      "v_and_b32 v12, 3, v1\n"
      "s_mov_b32 s40, s22\n"
      "s_and_b32 s41, s23, 0xffff\n"
      "v_lshrrev_b32 v104, 4, v1\n"
      "v_and_b32 v105, 15, v1\n"
      "v_and_b32 v106, 48, v1\n"
      "v_bfe_u32 v107, v1, 2, 2\n"
      "s_lshl_b32 s44, s35, 10\n"
      "s_add_u32 s44, s44, 4096\n"
      "v_add_u32 v109, s44, v2\n"
      "s_lshr_b32 s46, s32, 3\n"
      "s_lshl_b32 s45, s46, 8\n"
      "s_add_u32 s45, s45, s44\n"
      "v_add_u32 v110, s45, v106\n"
      "v_lshl_or_b32 v13, v104, 3, v107\n"
      "v_bfe_u32 v14, v1, 3, 2\n"
      "v_xor_b32 v14, s35, v14\n"
      "v_lshlrev_b32 v14, 5, v14\n"
      "v_lshl_or_b32 v14, v12, 3, v14\n"
      "v_lshl_or_b32 v111, v13, 7, v14\n"
      "s_lshl_b32 s47, s31, 14\n"
      "s_lshl_b32 s48, s32, 9\n"
      "s_add_u32 s48, s48, s47\n"
      "s_lshl_b32 s49, s35, 7\n"
      "s_add_u32 s48, s48, s49\n"
      "s_add_u32 s63, s48, 4096\n"
      "v_lshlrev_b32 v112, 3, v1\n"
      "v_cmp_gt_u32 vcc, 16, v1\n"
      "v_bfrev_b32 v113, -2\n"
      "v_and_b32 v15, 1, v1\n"
      "v_cndmask_b32 v112, v113, v112, vcc\n"
      "v_cmp_eq_u32 s[38:39], 0, v15\n"
      "s_lshl_b32 s44, s31, 16\n"
      "s_lshl_b32 s45, s35, 12\n"
      "s_add_u32 s45, s45, s44\n"
      "s_lshl_b32 s46, s32, 7\n"
      "s_add_u32 s45, s45, s46\n"
      "s_add_u32 s45, s45, 266240\n"
      "v_lshlrev_b32 v14, 3, v105\n"
      "v_lshl_or_b32 v14, v104, 14, v14\n"
      "v_add_u32 v136, s45, v14\n"
      "s_lshl_b32 s44, s31, 15\n"
      "s_lshl_b32 s45, s33, 12\n"
      "s_add_u32 s44, s44, s45\n"
      "s_lshl_b32 s45, s34, 10\n"
      "s_add_u32 s44, s44, s45\n"
      "s_lshl_b32 s45, s35, 8\n"
      "s_add_u32 s44, s44, s45\n"
      "s_add_u32 s44, s44, 790528\n"
      "v_add_u32 v14, s44, v2\n"
      "v_cmp_gt_u32 vcc, 16, v1\n"
      "s_nop 1\n"
      "v_cndmask_b32 v138, v113, v14, vcc\n"
      "s_waitcnt vmcnt(0)\n"
      "v_lshlrev_b32 v16, 16, v4\n"
      "v_and_b32 v17, 0xffff0000, v4\n"
      "v_lshlrev_b32 v18, 16, v5\n"
      "v_and_b32 v19, 0xffff0000, v5\n"
      "v_lshlrev_b32 v20, 16, v6\n"
      "v_and_b32 v21, 0xffff0000, v6\n"
      "v_lshlrev_b32 v22, 16, v7\n"
      "v_and_b32 v23, 0xffff0000, v7\n"
      "v_pk_add_f32 v[24:25], v[16:17], v[18:19]\n"
      "v_pk_add_f32 v[26:27], v[20:21], v[22:23]\n"
      "v_pk_add_f32 v[24:25], v[24:25], v[26:27]\n"
      "v_add_f32 v24, v24, v25\n"
      "s_nop 1\n"
      "v_add_f32_dpp v24, v24, v24 quad_perm:[1,0,3,2] row_mask:0xf bank_mask:0xf bound_ctrl:1\n"
      "s_nop 1\n"
      "v_add_f32_dpp v24, v24, v24 quad_perm:[2,3,0,1] row_mask:0xf bank_mask:0xf bound_ctrl:1\n"
      "s_nop 1\n"
      "v_add_f32_dpp v24, v24, v24 row_half_mirror row_mask:0xf bank_mask:0xf bound_ctrl:1\n"
      "s_nop 1\n"
      "v_add_f32_dpp v24, v24, v24 row_ror:8 row_mask:0xf bank_mask:0xf bound_ctrl:1\n"
      "v_mov_b32 v25, v24\n"
      "s_nop 1\n"
      "v_permlane16_swap_b32 v24, v25\n"
      "v_add_f32 v24, v24, v25\n"
      "v_mov_b32 v25, v24\n"
      "s_nop 1\n"
      "v_permlane32_swap_b32 v24, v25\n"
      "v_add_f32 v24, v24, v25\n"
      "v_mul_f32 v24, 0x3b000000, v24\n"
      "v_pk_add_f32 v[26:27], v[16:17], v[24:25] op_sel_hi:[1,0] neg_lo:[0,1] neg_hi:[0,1]\n"
      "v_pk_add_f32 v[28:29], v[18:19], v[24:25] op_sel_hi:[1,0] neg_lo:[0,1] neg_hi:[0,1]\n"
      "v_pk_add_f32 v[30:31], v[20:21], v[24:25] op_sel_hi:[1,0] neg_lo:[0,1] neg_hi:[0,1]\n"
      "v_pk_add_f32 v[4:5], v[22:23], v[24:25] op_sel_hi:[1,0] neg_lo:[0,1] neg_hi:[0,1]\n"
      "v_pk_mul_f32 v[26:27], v[26:27], v[26:27]\n"
      "v_pk_mul_f32 v[30:31], v[30:31], v[30:31]\n"
      "v_pk_fma_f32 v[26:27], v[28:29], v[28:29], v[26:27]\n"
      "v_pk_fma_f32 v[30:31], v[4:5], v[4:5], v[30:31]\n"
      "v_pk_add_f32 v[26:27], v[26:27], v[30:31]\n"
      "v_add_f32 v26, v26, v27\n"
      "v_cvt_pk_bf16_f32 v24, v24, v24\n"
      "v_lshlrev_b32 v24, 16, v24\n"
      "v_add_f32_dpp v26, v26, v26 quad_perm:[1,0,3,2] row_mask:0xf bank_mask:0xf bound_ctrl:1\n"
      "v_pk_add_f32 v[16:17], v[16:17], v[24:25] op_sel_hi:[1,0] neg_lo:[0,1] neg_hi:[0,1]\n"
      "v_cvt_pk_bf16_f32 v28, v16, v17\n"
      "v_add_f32_dpp v26, v26, v26 quad_perm:[2,3,0,1] row_mask:0xf bank_mask:0xf bound_ctrl:1\n"
      "v_pk_add_f32 v[18:19], v[18:19], v[24:25] op_sel_hi:[1,0] neg_lo:[0,1] neg_hi:[0,1]\n"
      "v_cvt_pk_bf16_f32 v29, v18, v19\n"
      "v_add_f32_dpp v26, v26, v26 row_half_mirror row_mask:0xf bank_mask:0xf bound_ctrl:1\n"
      "v_pk_add_f32 v[20:21], v[20:21], v[24:25] op_sel_hi:[1,0] neg_lo:[0,1] neg_hi:[0,1]\n"
      "v_cvt_pk_bf16_f32 v30, v20, v21\n"
      "v_add_f32_dpp v26, v26, v26 row_ror:8 row_mask:0xf bank_mask:0xf bound_ctrl:1\n"
      "v_pk_add_f32 v[22:23], v[22:23], v[24:25] op_sel_hi:[1,0] neg_lo:[0,1] neg_hi:[0,1]\n"
      "v_cvt_pk_bf16_f32 v31, v22, v23\n"
      "v_mov_b32 v27, v26\n"
      "v_lshlrev_b32 v16, 16, v28\n"
      "v_and_b32 v17, 0xffff0000, v28\n"
      "v_permlane16_swap_b32 v26, v27\n"
      "v_add_f32 v26, v26, v27\n"
      "v_mov_b32 v27, v26\n"
      "v_lshlrev_b32 v18, 16, v29\n"
      "v_and_b32 v19, 0xffff0000, v29\n"
      "v_permlane32_swap_b32 v26, v27\n"
      "v_add_f32 v26, v26, v27\n"
      "v_mul_f32 v26, 0x3b000000, v26\n"
      "v_cvt_pk_bf16_f32 v26, v26, v26\n"
      "v_lshlrev_b32 v26, 16, v26\n"
      "v_add_f32 v26, 0x3727c5ac, v26\n"
      "v_cvt_pk_bf16_f32 v26, v26, v26\n"
      "v_lshlrev_b32 v26, 16, v26\n"
      "v_sqrt_f32 v26, v26\n"
      "v_lshlrev_b32 v20, 16, v30\n"
      "v_cvt_pk_bf16_f32 v26, v26, v26\n"
      "v_lshlrev_b32 v26, 16, v26\n"
      "v_rcp_f32 v26, v26\n"
      "v_and_b32 v21, 0xffff0000, v30\n"
      "v_lshlrev_b32 v22, 16, v31\n"
      "v_and_b32 v23, 0xffff0000, v31\n"
      "v_pk_mul_f32 v[16:17], v[16:17], v[26:27] op_sel_hi:[1,0]\n"
      "v_pk_mul_f32 v[18:19], v[18:19], v[26:27] op_sel_hi:[1,0]\n"
      "v_pk_mul_f32 v[20:21], v[20:21], v[26:27] op_sel_hi:[1,0]\n"
      "v_pk_mul_f32 v[22:23], v[22:23], v[26:27] op_sel_hi:[1,0]\n"
      "v_cvt_pk_bf16_f32 v4, v16, v17\n"
      "v_cvt_pk_bf16_f32 v5, v18, v19\n"
      "v_cvt_pk_bf16_f32 v6, v20, v21\n"
      "v_cvt_pk_bf16_f32 v7, v22, v23\n"
      "ds_write_b128 v109, v[4:7]\n"
      "ds_read_b128 v[32:35], v110 offset:0\n"
      "ds_read_b128 v[36:39], v110 offset:64\n"
      "ds_read_b128 v[40:43], v110 offset:128\n"
      "ds_read_b128 v[44:47], v110 offset:192\n"
      "s_waitcnt lgkmcnt(0)\n"
      "s_add_u32 s45, s3, 2\n"
      "s_and_b32 s45, s45, 0xfffe\n"
      "s_add_u32 s44, s3, 0x20000\n"
      "s_bitset1_b32 s44, 16\n"
      "s_lshr_b32 s37, s44, 16\n"
      "s_and_b32 s44, s44, 0xffff0000\n"
      "s_or_b32 s36, s44, s45\n"
      "v_mov_b32 v117, s36\n"
      "v_mov_b32 v118, s37\n"
      "v_mov_b32 v14, s52\n"
      "v_mov_b32 v116, s54\n"
      "v_cndmask_b32 v115, v118, v117, s[38:39]\n"
      "v_cndmask_b32 v116, v14, v116, s[38:39]\n"
      "s_mov_b32 s50, 8388608\n"
      "W5_%=:\n"
      "ds_read_b32 v14, v3 offset:8\n"
      "s_sub_u32 s50, s50, 1\n"
      "s_cbranch_scc1 WD6_%=\n"
      "s_waitcnt lgkmcnt(0)\n"
      "v_cmp_gt_u32 vcc, 1, v14\n"
      "s_cbranch_vccnz W5_%=\n"
      "WD6_%=:\n"
      "ds_read_b64_tr_b16 v[64:65], v111 offset:8192\n"
      "ds_read_b64_tr_b16 v[66:67], v111 offset:8704\n"
      "ds_read_b64_tr_b16 v[68:69], v111 offset:12288\n"
      "ds_read_b64_tr_b16 v[70:71], v111 offset:12800\n"
      "ds_read_b64_tr_b16 v[72:73], v111 offset:16384\n"
      "ds_read_b64_tr_b16 v[74:75], v111 offset:16896\n"
      "ds_read_b64_tr_b16 v[76:77], v111 offset:20480\n"
      "ds_read_b64_tr_b16 v[78:79], v111 offset:20992\n"
      "s_waitcnt lgkmcnt(6)\n"
      "v_mfma_f32_16x16x32_bf16 v[96:99], v[32:35], v[64:67], 0\n"
      "s_waitcnt lgkmcnt(4)\n"
      "v_mfma_f32_16x16x32_bf16 v[96:99], v[36:39], v[68:71], v[96:99]\n"
      "s_waitcnt lgkmcnt(2)\n"
      "v_mfma_f32_16x16x32_bf16 v[96:99], v[40:43], v[72:75], v[96:99]\n"
      "s_waitcnt lgkmcnt(0)\n"
      "v_mfma_f32_16x16x32_bf16 v[96:99], v[44:47], v[76:79], v[96:99]\n"
      "s_nop 7\n"
      "v_mov_b32 v97, v117\n"
      "buffer_store_dwordx2 v[96:97], v112, s[40:43], s63 offen sc1\n"
      "v_mov_b32 v140, 1\n"
      "s_mov_b64 exec, 1\n"
      "ds_add_u32 v3, v140 offset:24\n"
      "s_mov_b64 exec, -1\n"
      "s_branch PAGE1_%=\n"
      "POLLER_%=:\n"
      "s_waitcnt lgkmcnt(0)\n"
      "s_add_u32 s45, s3, 2\n"
      "s_and_b32 s45, s45, 0xfffe\n"
      "s_add_u32 s44, s3, 0x20000\n"
      "s_bitset1_b32 s44, 16\n"
      "s_lshr_b32 s37, s44, 16\n"
      "s_and_b32 s44, s44, 0xffff0000\n"
      "s_or_b32 s36, s44, s45\n"
      "s_mov_b32 s50, 8388608\n"
      "W7_%=:\n"
      "s_sleep 1\n"
      "ds_read_b32 v14, v3 offset:20\n"
      "s_sub_u32 s50, s50, 1\n"
      "s_cbranch_scc1 WD8_%=\n"
      "s_waitcnt lgkmcnt(0)\n"
      "v_cmp_gt_u32 vcc, 1, v14\n"
      "s_cbranch_vccnz W7_%=\n"
      "WD8_%=:\n"
      "v_readfirstlane_b32 s44, v14\n"
      "s_sub_u32 s4, s4, 0x400\n"
      "s_add_u32 s4, s4, s44\n"
      "T9_%=:\n"
      "s_memrealtime s[6:7]\n"
      "s_waitcnt lgkmcnt(0)\n"
      "s_sub_u32 s6, s6, s4\n"
      "s_cmp_lt_i32 s6, 262\n"
      "s_cbranch_scc1 T9_%=\n"
      "s_mov_b32 s50, 524288\n"
      "GC10_%=:\n"
      "buffer_load_dwordx4 v[64:67], v16, s[40:43], 0 offen sc1\n"
      "buffer_load_dwordx4 v[68:71], v16, s[40:43], s59 offen sc1\n"
      "buffer_load_dwordx4 v[72:75], v16, s[40:43], s60 offen sc1\n"
      "buffer_load_dwordx4 v[76:79], v16, s[40:43], s61 offen sc1\n"
      "buffer_load_dwordx4 v[80:83], v16, s[40:43], 0 offen offset:1024 sc1\n"
      "buffer_load_dwordx4 v[84:87], v16, s[40:43], s59 offen offset:1024 sc1\n"
      "buffer_load_dwordx4 v[88:91], v16, s[40:43], s60 offen offset:1024 sc1\n"
      "buffer_load_dwordx4 v[92:95], v16, s[40:43], s61 offen offset:1024 sc1\n"
      "buffer_load_dwordx4 v[96:99], v16, s[40:43], 0 offen offset:2048 sc1\n"
      "buffer_load_dwordx4 v[100:103], v16, s[40:43], s59 offen offset:2048 sc1\n"
      "buffer_load_dwordx4 v[104:107], v16, s[40:43], s60 offen offset:2048 sc1\n"
      "buffer_load_dwordx4 v[108:111], v16, s[40:43], s61 offen offset:2048 sc1\n"
      "buffer_load_dwordx4 v[112:115], v16, s[40:43], 0 offen offset:3072 sc1\n"
      "buffer_load_dwordx4 v[116:119], v16, s[40:43], s59 offen offset:3072 sc1\n"
      "buffer_load_dwordx4 v[120:123], v16, s[40:43], s60 offen offset:3072 sc1\n"
      "buffer_load_dwordx4 v[124:127], v16, s[40:43], s61 offen offset:3072 sc1\n"
      "s_waitcnt vmcnt(14)\n"
      "v_bitop3_b32 v20, v65, v67, s36 bitop3:0x7e\n"
      "v_bitop3_b32 v21, v69, v71, s36 bitop3:0x7e\n"
      "v_or_b32 v20, v20, v21\n"
      "v_add_f32 v64, v64, v68\n"
      "v_add_f32 v66, v66, v70\n"
      "s_waitcnt vmcnt(12)\n"
      "v_bitop3_b32 v21, v73, v75, s36 bitop3:0x7e\n"
      "v_bitop3_b32 v22, v77, v79, s36 bitop3:0x7e\n"
      "v_or3_b32 v20, v20, v21, v22\n"
      "v_add_f32 v72, v72, v76\n"
      "v_add_f32 v74, v74, v78\n"
      "v_add_f32 v64, v64, v72\n"
      "v_add_f32 v66, v66, v74\n"
      "v_cvt_pk_bf16_f32 v65, v64, v66\n"
      "ds_write_b32 v18, v65 offset:1024\n"
      "s_waitcnt vmcnt(10)\n"
      "v_bitop3_b32 v21, v81, v83, s36 bitop3:0x7e\n"
      "v_bitop3_b32 v22, v85, v87, s36 bitop3:0x7e\n"
      "v_or3_b32 v20, v20, v21, v22\n"
      "v_add_f32 v80, v80, v84\n"
      "v_add_f32 v82, v82, v86\n"
      "s_waitcnt vmcnt(8)\n"
      "v_bitop3_b32 v21, v89, v91, s36 bitop3:0x7e\n"
      "v_bitop3_b32 v22, v93, v95, s36 bitop3:0x7e\n"
      "v_or3_b32 v20, v20, v21, v22\n"
      "v_add_f32 v88, v88, v92\n"
      "v_add_f32 v90, v90, v94\n"
      "v_add_f32 v80, v80, v88\n"
      "v_add_f32 v82, v82, v90\n"
      "v_cvt_pk_bf16_f32 v81, v80, v82\n"
      "ds_write_b32 v18, v81 offset:1344\n"
      "s_waitcnt vmcnt(6)\n"
      "v_bitop3_b32 v21, v97, v99, s36 bitop3:0x7e\n"
      "v_bitop3_b32 v22, v101, v103, s36 bitop3:0x7e\n"
      "v_or3_b32 v20, v20, v21, v22\n"
      "v_add_f32 v96, v96, v100\n"
      "v_add_f32 v98, v98, v102\n"
      "s_waitcnt vmcnt(4)\n"
      "v_bitop3_b32 v21, v105, v107, s36 bitop3:0x7e\n"
      "v_bitop3_b32 v22, v109, v111, s36 bitop3:0x7e\n"
      "v_or3_b32 v20, v20, v21, v22\n"
      "v_add_f32 v104, v104, v108\n"
      "v_add_f32 v106, v106, v110\n"
      "v_add_f32 v96, v96, v104\n"
      "v_add_f32 v98, v98, v106\n"
      "v_cvt_pk_bf16_f32 v97, v96, v98\n"
      "ds_write_b32 v18, v97 offset:1664\n"
      "s_waitcnt vmcnt(2)\n"
      "v_bitop3_b32 v21, v113, v115, s36 bitop3:0x7e\n"
      "v_bitop3_b32 v22, v117, v119, s36 bitop3:0x7e\n"
      "v_or3_b32 v20, v20, v21, v22\n"
      "v_add_f32 v112, v112, v116\n"
      "v_add_f32 v114, v114, v118\n"
      "s_waitcnt vmcnt(0)\n"
      "v_bitop3_b32 v21, v121, v123, s36 bitop3:0x7e\n"
      "v_bitop3_b32 v22, v125, v127, s36 bitop3:0x7e\n"
      "v_or3_b32 v20, v20, v21, v22\n"
      "v_add_f32 v120, v120, v124\n"
      "v_add_f32 v122, v122, v126\n"
      "v_cmp_ne_u32 vcc, 0, v20\n"
      "v_add_f32 v112, v112, v120\n"
      "v_add_f32 v114, v114, v122\n"
      "v_cvt_pk_bf16_f32 v113, v112, v114\n"
      "ds_write_b32 v18, v113 offset:1984\n"
      "s_cbranch_vccnz GR12_%=\n"
      "GOK11_%=:\n"
      "ds_write_b32 v3, v61 offset:4\n"
      "s_waitcnt lgkmcnt(0)\n"
      "s_endpgm\n"
      "GR12_%=:\n"
      "s_sub_u32 s50, s50, 1\n"
      "s_cbranch_scc0 GC10_%=\n"
      "s_waitcnt vmcnt(0)\n"
      "s_branch GOK11_%=\n"
      ".p2align 12\n"
      "PAD1_%=:\n"
      "s_branch TAIL_%=\n"
      "PAGE1_%=:\n"
      "s_lshl_b32 s44, s35, 13\n"
      "s_add_u32 s44, s44, 32768\n"
      "v_bfe_u32 v14, v1, 1, 2\n"
      "v_lshl_or_b32 v14, v15, 3, v14\n"
      "v_xor_b32 v14, v14, v104\n"
      "v_lshlrev_b32 v14, 4, v14\n"
      "v_lshl_or_b32 v14, v105, 9, v14\n"
      "v_add_u32 v120, s44, v14\n"
      "v_xor_b32 v124, 0x140, v120\n"
      "v_xor_b32 v121, 64, v120\n"
      "v_xor_b32 v122, 128, v120\n"
      "v_xor_b32 v123, 192, v120\n"
      "v_xor_b32 v125, 64, v124\n"
      "v_xor_b32 v126, 128, v124\n"
      "v_xor_b32 v127, 192, v124\n"
      "v_mul_u32_u24 v128, 320, v107\n"
      "v_add_u32 v128, v128, v106\n"
      "v_mul_u32_u24 v129, 320, v104\n"
      "v_lshl_add_u32 v129, v105, 2, v129\n"
      "s_lshl_b32 s44, s35, 6\n"
      "v_add_u32 v129, s44, v129\n"
      "v_lshlrev_b32 v131, 5, v104\n"
      "s_lshl_b32 s44, s35, 2\n"
      "v_lshl_add_u32 v143, v15, 4, s44\n"
      "v_add_u32 v130, v131, v143\n"
      "v_bfe_u32 v14, v1, 1, 1\n"
      "v_or_b32 v14, s44, v14\n"
      "v_and_b32 v16, 12, v1\n"
      "v_xor_b32 v14, v14, v16\n"
      "v_lshrrev_b32 v16, 3, v1\n"
      "v_and_b32 v16, 2, v16\n"
      "v_xor_b32 v14, v14, v16\n"
      "v_lshlrev_b32 v14, 4, v14\n"
      "v_lshl_or_b32 v14, v15, 3, v14\n"
      "v_lshl_or_b32 v14, v13, 8, v14\n"
      "v_add_u32 v132, 65536, v14\n"
      "v_xor_b32 v133, 16, v132\n"
      "v_add_u32 v133, 0x400, v133\n"
      "v_xor_b32 v134, 32, v132\n"
      "v_xor_b32 v135, 32, v133\n"
      "s_lshl_b32 s44, s31, 16\n"
      "s_lshl_b32 s45, s33, 13\n"
      "s_add_u32 s45, s45, s44\n"
      "s_lshl_b32 s46, s35, 10\n"
      "s_add_u32 s45, s45, s46\n"
      "s_add_u32 s45, s45, 266240\n"
      "v_add_u32 v137, s45, v2\n"
      "s_movk_i32 s59, 0x1000\n"
      "s_mul_i32 s44, s35, 144\n"
      "v_and_b32 v16, 7, v1\n"
      "v_lshl_or_b32 v16, v104, 3, v16\n"
      "v_lshl_add_u32 v139, v16, 2, s44\n"
      "v_mul_u32_u24 v141, 144, v12\n"
      "v_add_u32 v141, v141, v106\n"
      "s_lshl_b32 s44, s31, 5\n"
      "s_lshl_b32 s45, s32, 2\n"
      "s_or_b32 s44, s44, s45\n"
      "s_and_b32 s44, s44, 0xfc\n"
      "v_or_b32 v108, s44, v12\n"
      "v_and_b32 v16, 15, v108\n"
      "v_lshrrev_b32 v14, 4, v108\n"
      "v_lshl_or_b32 v16, v14, 5, v16\n"
      "v_lshrrev_b32 v14, 5, v1\n"
      "v_lshl_or_b32 v16, v14, 4, v16\n"
      "v_lshlrev_b32 v11, 1, v16\n"
      "v_lshrrev_b32 v14, 2, v1\n"
      "v_lshl_or_b32 v14, v14, 8, v108\n"
      "v_lshlrev_b32 v14, 4, v14\n"
      "v_add_u32 v142, 790528, v14\n"
      "s_mov_b32 s60, 0x10000\n"
      "s_mov_b32 s61, 0x20000\n"
      "v_mov_b32 v140, 1\n"
      "s_mov_b32 s62, 0x30000\n"
      "s_lshl_b32 s58, s35, 1\n"
      "s_add_u32 s58, s58, 8\n"
      "s_mov_b32 s50, 8388608\n"
      "W13_%=:\n"
      "s_sleep 1\n"
      "ds_read_b32 v14, v3 offset:0\n"
      "s_sub_u32 s50, s50, 1\n"
      "s_cbranch_scc1 WD14_%=\n"
      "s_waitcnt lgkmcnt(0)\n"
      "v_cmp_gt_u32 vcc, s58, v14\n"
      "s_cbranch_vccnz W13_%=\n"
      "WD14_%=:\n"
      "ds_read_b128 v[32:35], v120\n"
      "ds_read_b128 v[36:39], v121\n"
      "ds_read_b128 v[40:43], v122\n"
      "ds_read_b128 v[44:47], v123\n"
      "ds_read_b128 v[48:51], v124\n"
      "ds_read_b128 v[52:55], v125\n"
      "ds_read_b128 v[56:59], v126\n"
      "ds_read_b128 v[60:63], v127\n"
      "s_mov_b32 s50, 8388608\n"
      "W15_%=:\n"
      "s_sleep 1\n"
      "ds_read_b32 v14, v3 offset:20\n"
      "s_sub_u32 s50, s50, 1\n"
      "s_cbranch_scc1 WD16_%=\n"
      "s_waitcnt lgkmcnt(0)\n"
      "v_cmp_gt_u32 vcc, 1, v14\n"
      "s_cbranch_vccnz W15_%=\n"
      "WD16_%=:\n"
      "v_readfirstlane_b32 s44, v14\n"
      "s_sub_u32 s4, s4, 0x400\n"
      "s_add_u32 s4, s4, s44\n"
      "s_mov_b32 s50, 8388608\n"
      "W17_%=:\n"
      "ds_read_b32 v14, v3 offset:4\n"
      "s_sub_u32 s50, s50, 1\n"
      "s_cbranch_scc1 WD18_%=\n"
      "s_waitcnt lgkmcnt(0)\n"
      "v_cmp_gt_u32 vcc, 1, v14\n"
      "s_cbranch_vccnz W17_%=\n"
      "WD18_%=:\n"
      "s_setprio 2\n"
      "ds_read_b128 v[64:67], v128 offset:1024\n"
      "ds_read_b128 v[68:71], v128 offset:1088\n"
      "ds_read_b128 v[72:75], v128 offset:1152\n"
      "ds_read_b128 v[76:79], v128 offset:1216\n"
      "s_waitcnt lgkmcnt(3)\n"
      "v_mfma_f32_16x16x32_bf16 v[96:99], v[64:67], v[32:35], 0\n"
      "v_mfma_f32_16x16x32_bf16 v[100:103], v[64:67], v[48:51], 0\n"
      "s_waitcnt lgkmcnt(2)\n"
      "v_mfma_f32_16x16x32_bf16 v[96:99], v[68:71], v[36:39], v[96:99]\n"
      "v_mfma_f32_16x16x32_bf16 v[100:103], v[68:71], v[52:55], v[100:103]\n"
      "s_waitcnt lgkmcnt(1)\n"
      "v_mfma_f32_16x16x32_bf16 v[96:99], v[72:75], v[40:43], v[96:99]\n"
      "v_mfma_f32_16x16x32_bf16 v[100:103], v[72:75], v[56:59], v[100:103]\n"
      "s_waitcnt lgkmcnt(0)\n"
      "v_mfma_f32_16x16x32_bf16 v[96:99], v[76:79], v[44:47], v[96:99]\n"
      "v_mfma_f32_16x16x32_bf16 v[100:103], v[76:79], v[60:63], v[100:103]\n"
      "s_nop 7\n"
      "v_cvt_pk_bf16_f32 v16, v96, v100\n"
      "v_lshlrev_b32 v17, 16, v16\n"
      "v_and_b32 v16, 0xffff0000, v16\n"
      "v_mul_f32 v17, 0x3db504f3, v17\n"
      "v_mul_f32 v16, 0x3db504f3, v16\n"
      "v_cvt_pk_bf16_f32 v16, v17, v16\n"
      "v_lshlrev_b32 v17, 16, v16\n"
      "v_and_b32 v16, 0xffff0000, v16\n"
      "v_max_f32 v18, v17, v16\n"
      "s_nop 1\n"
      "v_max_f32_dpp v18, v18, v18 quad_perm:[1,0,3,2] row_mask:0xf bank_mask:0xf bound_ctrl:1\n"
      "s_nop 1\n"
      "v_max_f32_dpp v18, v18, v18 quad_perm:[2,3,0,1] row_mask:0xf bank_mask:0xf bound_ctrl:1\n"
      "s_nop 1\n"
      "v_max_f32_dpp v18, v18, v18 row_half_mirror row_mask:0xf bank_mask:0xf bound_ctrl:1\n"
      "s_nop 1\n"
      "v_max_f32_dpp v18, v18, v18 row_ror:8 row_mask:0xf bank_mask:0xf bound_ctrl:1\n"
      "v_sub_f32 v17, v17, v18\n"
      "v_sub_f32 v16, v16, v18\n"
      "v_mul_f32 v17, 0x3fb8aa3b, v17\n"
      "v_mul_f32 v16, 0x3fb8aa3b, v16\n"
      "v_exp_f32 v17, v17\n"
      "v_exp_f32 v16, v16\n"
      "v_mul_f32 v18, 0x3fb8aa3b, v18\n"
      "v_cvt_pk_bf16_f32 v16, v17, v16\n"
      "ds_write_b32 v129, v16 offset:2304\n"
      "v_lshlrev_b32 v17, 16, v16\n"
      "v_and_b32 v16, 0xffff0000, v16\n"
      "v_add_f32 v16, v17, v16\n"
      "s_nop 1\n"
      "v_add_f32_dpp v16, v16, v16 quad_perm:[1,0,3,2] row_mask:0xf bank_mask:0xf bound_ctrl:1\n"
      "s_nop 1\n"
      "v_add_f32_dpp v16, v16, v16 quad_perm:[2,3,0,1] row_mask:0xf bank_mask:0xf bound_ctrl:1\n"
      "s_nop 1\n"
      "v_add_f32_dpp v16, v16, v16 row_half_mirror row_mask:0xf bank_mask:0xf bound_ctrl:1\n"
      "s_nop 1\n"
      "v_add_f32_dpp v16, v16, v16 row_ror:8 row_mask:0xf bank_mask:0xf bound_ctrl:1\n"
      "v_cndmask_b32 v16, v16, v18, s[38:39]\n"
      "ds_write_b32 v130, v16 offset:128\n"
      "s_mov_b64 exec, 1\n"
      "ds_add_u32 v3, v140 offset:12\n"
      "s_mov_b64 exec, -1\n"
      "s_mov_b32 s50, 8388608\n"
      "W19_%=:\n"
      "ds_read_b32 v14, v3 offset:12\n"
      "s_sub_u32 s50, s50, 1\n"
      "s_cbranch_scc1 WD20_%=\n"
      "s_waitcnt lgkmcnt(0)\n"
      "v_cmp_gt_u32 vcc, 5, v14\n"
      "s_cbranch_vccnz W19_%=\n"
      "WD20_%=:\n"
      "ds_read_b128 v[80:83], v131 offset:128\n"
      "ds_read_b128 v[84:87], v131 offset:144\n"
      "ds_read_b128 v[32:35], v128 offset:2304\n"
      "ds_read_b64_tr_b16 v[48:49], v132 offset:0\n"
      "ds_read_b64_tr_b16 v[50:51], v133 offset:0\n"
      "ds_read_b64_tr_b16 v[64:65], v134 offset:0\n"
      "ds_read_b64_tr_b16 v[66:67], v135 offset:0\n"
      "ds_read_b128 v[36:39], v128 offset:2368\n"
      "ds_read_b64_tr_b16 v[52:53], v132 offset:8192\n"
      "ds_read_b64_tr_b16 v[54:55], v133 offset:8192\n"
      "ds_read_b64_tr_b16 v[68:69], v134 offset:8192\n"
      "ds_read_b64_tr_b16 v[70:71], v135 offset:8192\n"
      "ds_read_b128 v[40:43], v128 offset:2432\n"
      "ds_read_b64_tr_b16 v[56:57], v132 offset:16384\n"
      "ds_read_b64_tr_b16 v[58:59], v133 offset:16384\n"
      "ds_read_b64_tr_b16 v[72:73], v134 offset:16384\n"
      "ds_read_b64_tr_b16 v[74:75], v135 offset:16384\n"
      "ds_read_b128 v[44:47], v128 offset:2496\n"
      "ds_read_b64_tr_b16 v[60:61], v132 offset:24576\n"
      "ds_read_b64_tr_b16 v[62:63], v133 offset:24576\n"
      "ds_read_b64_tr_b16 v[76:77], v134 offset:24576\n"
      "ds_read_b64_tr_b16 v[78:79], v135 offset:24576\n"
      "s_waitcnt lgkmcnt(15)\n"
      "v_mfma_f32_16x16x32_bf16 v[144:147], v[32:35], v[48:51], 0\n"
      "v_mfma_f32_16x16x32_bf16 v[148:151], v[32:35], v[64:67], 0\n"
      "v_max3_f32 v16, v80, v81, v82\n"
      "v_max_f32 v16, v16, v83\n"
      "v_sub_f32 v80, v80, v16\n"
      "v_sub_f32 v81, v81, v16\n"
      "v_sub_f32 v82, v82, v16\n"
      "v_sub_f32 v83, v83, v16\n"
      "s_waitcnt lgkmcnt(10)\n"
      "v_mfma_f32_16x16x32_bf16 v[152:155], v[36:39], v[52:55], 0\n"
      "v_mfma_f32_16x16x32_bf16 v[156:159], v[36:39], v[68:71], 0\n"
      "v_exp_f32 v80, v80\n"
      "v_exp_f32 v81, v81\n"
      "v_exp_f32 v82, v82\n"
      "v_exp_f32 v83, v83\n"
      "s_waitcnt lgkmcnt(5)\n"
      "v_mfma_f32_16x16x32_bf16 v[160:163], v[40:43], v[56:59], 0\n"
      "v_mfma_f32_16x16x32_bf16 v[164:167], v[40:43], v[72:75], 0\n"
      "v_pk_mul_f32 v[84:85], v[80:81], v[84:85]\n"
      "v_pk_fma_f32 v[84:85], v[82:83], v[86:87], v[84:85]\n"
      "v_add_f32 v84, v84, v85\n"
      "s_waitcnt lgkmcnt(0)\n"
      "v_mfma_f32_16x16x32_bf16 v[168:171], v[44:47], v[60:63], 0\n"
      "v_mfma_f32_16x16x32_bf16 v[172:175], v[44:47], v[76:79], 0\n"
      "v_log_f32 v85, v84\n"
      "v_rcp_f32 v84, v84\n"
      "v_mul_f32 v20, v80, v144\n"
      "v_mul_f32 v18, v80, v148\n"
      "v_add_f32 v85, v85, v16\n"
      "v_fmac_f32 v20, v81, v152\n"
      "v_fmac_f32 v18, v81, v156\n"
      "v_perm_b32 v21, v85, v115, v116\n"
      "v_fmac_f32 v20, v82, v160\n"
      "v_fmac_f32 v18, v82, v164\n"
      "v_fmac_f32 v20, v83, v168\n"
      "v_fmac_f32 v18, v83, v172\n"
      "v_mul_f32 v20, v20, v84\n"
      "v_mul_f32 v18, v18, v84\n"
      "v_cvt_pk_bf16_f32 v20, v20, v18\n"
      "buffer_store_dwordx2 v[20:21], v136, s[40:43], 0 offen sc1\n"
      "s_lshr_b32 s44, s2, 6\n"
      "s_or_b32 s44, s44, s35\n"
      "s_cbranch_scc1 NOAUX_%=\n"
      "global_load_ushort v9, v11, s[20:21]\n"
      "global_load_ushort v10, v11, s[24:25]\n"
      "NOAUX_%=:\n"
      "s_sleep 18\n"
      "T21_%=:\n"
      "s_memrealtime s[6:7]\n"
      "s_waitcnt lgkmcnt(0)\n"
      "s_sub_u32 s6, s6, s4\n"
      "s_cmp_lt_i32 s6, 477\n"
      "s_cbranch_scc1 T21_%=\n"
      "buffer_load_dwordx4 v[68:71], v137, s[40:43], 0 offen sc1\n"
      "buffer_load_dwordx4 v[72:75], v137, s[40:43], s59 offen sc1\n"
      "s_mov_b32 s50, 524288\n"
      "GC22_%=:\n"
      "s_waitcnt vmcnt(1)\n"
      "v_perm_b32 v16, v71, v69, s52\n"
      "s_waitcnt vmcnt(0)\n"
      "v_perm_b32 v17, v75, v73, s52\n"
      "v_bitop3_b32 v16, v16, v17, s36 bitop3:0x7e\n"
      "v_cmp_ne_u32 vcc, 0, v16\n"
      "s_cbranch_vccnz GR24_%=\n"
      "GOK23_%=:\n"
      "ds_read_b32 v90, v3 offset:16\n"
      "v_perm_b32 v16, v69, v71, s56\n"
      "v_lshlrev_b32 v80, 16, v68\n"
      "v_lshlrev_b32 v81, 16, v70\n"
      "v_max_f32_dpp v17, v16, v16 row_ror:8 row_mask:0xf bank_mask:0xf bound_ctrl:1\n"
      "v_mov_b32 v18, v17\n"
      "v_and_b32 v82, 0xffff0000, v68\n"
      "v_and_b32 v83, 0xffff0000, v70\n"
      "v_permlane16_swap_b32 v17, v18\n"
      "v_max_f32 v17, v17, v18\n"
      "v_mov_b32 v18, v17\n"
      "v_lshlrev_b32 v84, 16, v72\n"
      "v_lshlrev_b32 v85, 16, v74\n"
      "v_permlane32_swap_b32 v17, v18\n"
      "v_max_f32 v17, v17, v18\n"
      "v_sub_f32 v16, v16, v17\n"
      "v_exp_f32 v88, v16\n"
      "v_and_b32 v86, 0xffff0000, v72\n"
      "v_and_b32 v87, 0xffff0000, v74\n"
      "v_pk_mul_f32 v[80:81], v[80:81], v[88:89] op_sel_hi:[1,0]\n"
      "v_pk_mul_f32 v[84:85], v[84:85], v[88:89] op_sel_hi:[1,0]\n"
      "v_pk_mul_f32 v[82:83], v[82:83], v[88:89] op_sel_hi:[1,0]\n"
      "v_pk_mul_f32 v[86:87], v[86:87], v[88:89] op_sel_hi:[1,0]\n"
      "v_permlane32_swap_b32 v80, v84\n"
      "v_permlane32_swap_b32 v81, v85\n"
      "v_add_f32_dpp v18, v88, v88 row_ror:8 row_mask:0xf bank_mask:0xf bound_ctrl:1\n"
      "v_permlane32_swap_b32 v82, v86\n"
      "v_permlane32_swap_b32 v83, v87\n"
      "v_pk_add_f32 v[80:81], v[80:81], v[84:85]\n"
      "v_pk_add_f32 v[82:83], v[82:83], v[86:87]\n"
      "v_mov_b32 v19, v18\n"
      "s_lshr_b32 s44, s2, 6\n"
      "v_permlane16_swap_b32 v80, v82\n"
      "v_permlane16_swap_b32 v81, v83\n"
      "v_permlane16_swap_b32 v18, v19\n"
      "v_pk_add_f32 v[80:81], v[80:81], v[82:83]\n"
      "v_add_f32 v18, v18, v19\n"
      "v_mov_b32 v19, v18\n"
      "v_add_f32_dpp v80, v80, v80 row_ror:8 row_mask:0xf bank_mask:0xf bound_ctrl:1\n"
      "v_add_f32_dpp v81, v81, v81 row_ror:8 row_mask:0xf bank_mask:0xf bound_ctrl:1\n"
      "v_permlane32_swap_b32 v18, v19\n"
      "v_cvt_pk_bf16_f32 v80, v80, v81\n"
      "v_add_f32 v18, v18, v19\n"
      "s_waitcnt lgkmcnt(0)\n"
      "ds_write_b32 v139, v80 offset:320\n"
      "v_cndmask_b32 v18, v18, v17, s[38:39]\n"
      "v_cmp_gt_u32 vcc, 3, v90\n"
      "ds_write_b32 v143, v18 offset:256\n"
      "s_or_b32 s44, s44, s35\n"
      "s_cbranch_vccnz WLATE_%=\n"
      "WOK_%=:\n"
      "ds_read_b64_tr_b16 v[48:49], v132 offset:49152\n"
      "ds_read_b64_tr_b16 v[50:51], v133 offset:49152\n"
      "ds_read_b64_tr_b16 v[52:53], v134 offset:49152\n"
      "ds_read_b64_tr_b16 v[54:55], v135 offset:49152\n"
      "ds_read_b64_tr_b16 v[32:33], v132 offset:32768\n"
      "ds_read_b64_tr_b16 v[34:35], v133 offset:32768\n"
      "ds_read_b64_tr_b16 v[36:37], v134 offset:32768\n"
      "ds_read_b64_tr_b16 v[38:39], v135 offset:32768\n"
      "ds_read_b64_tr_b16 v[40:41], v132 offset:40960\n"
      "ds_read_b64_tr_b16 v[42:43], v133 offset:40960\n"
      "ds_read_b64_tr_b16 v[44:45], v134 offset:40960\n"
      "ds_read_b64_tr_b16 v[46:47], v135 offset:40960\n"
      "s_waitcnt lgkmcnt(12)\n"
      "s_barrier\n"
      "ds_read_b128 v[80:83], v3 offset:256\n"
      "ds_read_b128 v[68:71], v141 offset:320\n"
      "ds_read_b128 v[72:75], v141 offset:384\n"
      "ds_read_b128 v[56:59], v106 offset:64\n"
      "ds_read_b128 v[84:87], v3 offset:272\n"
      "s_waitcnt lgkmcnt(3)\n"
      "v_mfma_f32_16x16x32_bf16 v[96:99], v[68:71], v[32:35], 0\n"
      "v_mfma_f32_16x16x32_bf16 v[100:103], v[68:71], v[36:39], 0\n"
      "v_max3_f32 v16, v80, v81, v82\n"
      "s_waitcnt lgkmcnt(2)\n"
      "v_mfma_f32_16x16x32_bf16 v[96:99], v[72:75], v[40:43], v[96:99]\n"
      "v_max_f32 v16, v16, v83\n"
      "v_sub_f32 v80, v80, v16\n"
      "v_mfma_f32_16x16x32_bf16 v[100:103], v[72:75], v[44:47], v[100:103]\n"
      "v_sub_f32 v81, v81, v16\n"
      "v_sub_f32 v82, v82, v16\n"
      "s_waitcnt lgkmcnt(1)\n"
      "v_mfma_f32_16x16x32_bf16 v[60:63], v[56:59], v[48:51], 0\n"
      "v_sub_f32 v83, v83, v16\n"
      "v_exp_f32 v80, v80\n"
      "v_mfma_f32_16x16x32_bf16 v[64:67], v[56:59], v[52:55], 0\n"
      "v_exp_f32 v81, v81\n"
      "v_exp_f32 v82, v82\n"
      "v_exp_f32 v83, v83\n"
      "s_waitcnt lgkmcnt(0)\n"
      "v_pk_mul_f32 v[84:85], v[80:81], v[84:85]\n"
      "v_pk_mul_f32 v[96:97], v[96:97], v[80:81]\n"
      "v_pk_fma_f32 v[84:85], v[82:83], v[86:87], v[84:85]\n"
      "v_pk_mul_f32 v[100:101], v[100:101], v[80:81]\n"
      "v_add_f32 v84, v84, v85\n"
      "v_rcp_f32 v84, v84\n"
      "v_pk_fma_f32 v[96:97], v[98:99], v[82:83], v[96:97]\n"
      "v_pk_fma_f32 v[100:101], v[102:103], v[82:83], v[100:101]\n"
      "v_perm_b32 v25, v60, v117, s54\n"
      "v_perm_b32 v27, v64, v118, s54\n"
      "v_add_f32 v96, v96, v97\n"
      "v_add_f32 v100, v100, v101\n"
      "v_mul_f32 v96, v96, v84\n"
      "v_mul_f32 v100, v100, v84\n"
      "v_perm_b32 v24, v96, v60, s53\n"
      "v_perm_b32 v26, v100, v64, s53\n"
      "buffer_store_dwordx4 v[24:27], v138, s[40:43], 0 offen sc1\n"
      "s_cmp_lg_u32 s44, 0\n"
      "s_cbranch_scc1 DONE_%=\n"
      "v_or_b32 v7, s2, v1\n"
      "v_cmp_eq_u32 vcc, 0, v7\n"
      "s_nop 1\n"
      "v_cndmask_b32 v7, v113, v3, vcc\n"
      "s_sleep 8\n"
      "s_mov_b32 s50, 524288\n"
      "GC25_%=:\n"
      "buffer_load_dwordx4 v[32:35], v142, s[40:43], 0 offen sc1\n"
      "buffer_load_dwordx4 v[36:39], v142, s[40:43], s60 offen sc1\n"
      "buffer_load_dwordx4 v[40:43], v142, s[40:43], s61 offen sc1\n"
      "buffer_load_dwordx4 v[44:47], v142, s[40:43], s62 offen sc1\n"
      "s_waitcnt vmcnt(3)\n"
      "v_perm_b32 v16, v35, v33, s52\n"
      "s_waitcnt vmcnt(2)\n"
      "v_perm_b32 v17, v39, v37, s52\n"
      "s_waitcnt vmcnt(1)\n"
      "v_perm_b32 v18, v43, v41, s52\n"
      "s_waitcnt vmcnt(0)\n"
      "v_perm_b32 v19, v47, v45, s52\n"
      "v_bitop3_b32 v16, v16, v17, s36 bitop3:0x7e\n"
      "v_bitop3_b32 v18, v18, v19, s36 bitop3:0x7e\n"
      "v_or_b32 v16, v16, v18\n"
      "v_cmp_ne_u32 vcc, 0, v16\n"
      "s_cbranch_vccnz GR27_%=\n"
      "GOK26_%=:\n"
      "buffer_store_dword v117, v7, s[40:43], 0 offen sc1\n"
      "v_perm_b32 v33, v33, v32, s55\n"
      "v_perm_b32 v35, v35, v34, s55\n"
      "v_perm_b32 v37, v37, v36, s55\n"
      "v_perm_b32 v39, v39, v38, s55\n"
      "v_perm_b32 v41, v41, v40, s55\n"
      "v_perm_b32 v43, v43, v42, s55\n"
      "v_perm_b32 v45, v45, v44, s55\n"
      "v_perm_b32 v47, v47, v46, s55\n"
      "v_pk_add_f32 v[32:33], v[32:33], v[36:37]\n"
      "v_pk_add_f32 v[34:35], v[34:35], v[38:39]\n"
      "v_pk_add_f32 v[40:41], v[40:41], v[44:45]\n"
      "v_pk_add_f32 v[42:43], v[42:43], v[46:47]\n"
      "v_pk_add_f32 v[32:33], v[32:33], v[40:41]\n"
      "v_pk_add_f32 v[34:35], v[34:35], v[42:43]\n"
      "v_lshlrev_b32 v9, 16, v9\n"
      "v_lshlrev_b32 v10, 16, v10\n"
      "v_permlane32_swap_b32 v32, v34\n"
      "v_permlane32_swap_b32 v33, v35\n"
      "v_pk_add_f32 v[32:33], v[32:33], v[34:35]\n"
      "v_mov_b32 v34, v32\n"
      "v_mov_b32 v35, v33\n"
      "s_nop 0\n"
      "v_permlane16_swap_b32 v32, v34\n"
      "v_permlane16_swap_b32 v33, v35\n"
      "v_pk_add_f32 v[32:33], v[32:33], v[34:35]\n"
      "s_nop 1\n"
      "v_add_f32_dpp v33, v33, v33 row_ror:8 row_mask:0xf bank_mask:0xf bound_ctrl:1\n"
      "v_add_f32_dpp v32, v32, v32 row_ror:8 row_mask:0xf bank_mask:0xf bound_ctrl:1\n"
      "s_nop 0\n"
      "v_add_f32_dpp v33, v33, v33 row_ror:4 row_mask:0xf bank_mask:0xf bound_ctrl:1\n"
      "v_add_f32_dpp v32, v32, v32 row_ror:4 row_mask:0xf bank_mask:0xf bound_ctrl:1\n"
      "v_cvt_pk_bf16_f32 v32, v32, v33\n"
      "v_and_b32 v33, 0xffff0000, v32\n"
      "v_add_f32 v33, v33, v10\n"
      "v_cvt_pk_bf16_f32 v33, v33, v33\n"
      "v_lshlrev_b32 v33, 16, v33\n"
      "v_mul_f32 v34, 0xbfb8aa3b, v33\n"
      "v_exp_f32 v34, v34\n"
      "v_lshlrev_b32 v32, 16, v32\n"
      "v_add_f32 v34, 1.0, v34\n"
      "v_rcp_f32 v34, v34\n"
      "s_mov_b32 exec_lo, 15\n"
      "v_mul_f32 v33, v33, v34\n"
      "s_mov_b32 exec_hi, 15\n"
      "v_cvt_pk_bf16_f32 v33, v33, v33\n"
      "v_lshlrev_b32 v33, 16, v33\n"
      "v_mul_f32 v32, v32, v33\n"
      "v_cvt_pk_bf16_f32 v32, v32, v32\n"
      "v_lshlrev_b32 v32, 16, v32\n"
      "v_add_f32 v32, v32, v9\n"
      "v_cvt_pk_bf16_f32 v32, v32, v32\n"
      "global_store_short v11, v32, s[26:27]\n"
      "DONE2_%=:\n"
      "DONE_%=:\n"
      "s_endpgm\n"
      "WLATE_%=:\n"
      "s_mov_b32 s50, 8388608\n"
      "W28_%=:\n"
      "ds_read_b32 v90, v3 offset:16\n"
      "s_sub_u32 s50, s50, 1\n"
      "s_cbranch_scc1 WD29_%=\n"
      "s_waitcnt lgkmcnt(0)\n"
      "v_cmp_gt_u32 vcc, 3, v90\n"
      "s_cbranch_vccnz W28_%=\n"
      "WD29_%=:\n"
      "s_branch WOK_%=\n"
      "GR24_%=:\n"
      "buffer_load_dwordx4 v[68:71], v137, s[40:43], 0 offen sc1\n"
      "buffer_load_dwordx4 v[72:75], v137, s[40:43], s59 offen sc1\n"
      "s_sub_u32 s50, s50, 1\n"
      "s_cbranch_scc0 GC22_%=\n"
      "s_waitcnt vmcnt(0)\n"
      "s_branch GOK23_%=\n"
      "GR27_%=:\n"
      "s_sub_u32 s50, s50, 1\n"
      "s_cbranch_scc0 GC25_%=\n"
      "s_waitcnt vmcnt(0)\n"
      "s_branch GOK26_%=\n"
      "TAIL_%=:\n"
      "s_sleep 20\n"
      "s_mov_b32 s50, 524288\n"
      "T30_%=:\n"
      "s_memrealtime s[6:7]\n"
      "s_waitcnt lgkmcnt(0)\n"
      "s_sub_u32 s6, s6, s4\n"
      "s_cmp_lt_i32 s6, 291\n"
      "s_cbranch_scc1 T30_%=\n"
      "s_mov_b32 m0, 24576\n"
      "s_nop 0\n"
      "global_load_lds_dwordx4 v32, s[10:11] nt\n"
      "global_load_lds_dwordx4 v33, s[10:11] offset:1024 nt\n"
      "global_load_lds_dwordx4 v34, s[10:11] offset:2048 nt\n"
      "global_load_lds_dwordx4 v35, s[10:11] offset:3072 nt\n"
      "s_mov_b32 m0, 28672\n"
      "s_add_u32 s10, s10, 0x1c000\n"
      "s_addc_u32 s11, s11, 0\n"
      "global_load_lds_dwordx4 v32, s[10:11] nt\n"
      "global_load_lds_dwordx4 v33, s[10:11] offset:1024 nt\n"
      "global_load_lds_dwordx4 v34, s[10:11] offset:2048 nt\n"
      "global_load_lds_dwordx4 v35, s[10:11] offset:3072 nt\n"
      "T31_%=:\n"
      "s_memrealtime s[6:7]\n"
      "s_waitcnt lgkmcnt(0)\n"
      "s_sub_u32 s6, s6, s4\n"
      "s_cmp_lt_i32 s6, 351\n"
      "s_cbranch_scc1 T31_%=\n"
      "s_mov_b32 s57, 4\n"
      "WOL_%=:\n"
      "s_lshr_b32 s62, s58, 2\n"
      "s_add_u32 m0, s62, 98304\n"
      "s_add_u32 s60, s16, s58\n"
      "s_addc_u32 s61, s17, 0\n"
      "s_add_u32 s58, s58, 0x4000\n"
      "s_and_b32 s58, s58, 0xc000\n"
      "s_sub_u32 s57, s57, 1\n"
      "s_cmp_lg_u32 s57, 0\n"
      "global_load_lds_dwordx4 v28, s[60:61] nt\n"
      "global_load_lds_dwordx4 v29, s[60:61] offset:1024 nt\n"
      "global_load_lds_dwordx4 v30, s[60:61] offset:2048 nt\n"
      "global_load_lds_dwordx4 v31, s[60:61] offset:3072 nt\n"
      "s_cbranch_scc1 WOL_%=\n"
      "s_mov_b32 m0, 114688\n"
      "s_nop 0\n"
      "global_load_lds_dwordx4 v28, s[18:19] nt\n"
      "global_load_lds_dwordx4 v29, s[18:19] offset:1024 nt\n"
      "global_load_lds_dwordx4 v30, s[18:19] offset:2048 nt\n"
      "global_load_lds_dwordx4 v31, s[18:19] offset:3072 nt\n"
      "s_mov_b32 m0, 118784\n"
      "s_add_u32 s18, s18, 0x4000\n"
      "s_addc_u32 s19, s19, 0\n"
      "global_load_lds_dwordx4 v28, s[18:19] nt\n"
      "global_load_lds_dwordx4 v29, s[18:19] offset:1024 nt\n"
      "global_load_lds_dwordx4 v30, s[18:19] offset:2048 nt\n"
      "global_load_ushort v43, v11, s[24:25]\n"
      "s_mov_b32 s50, 8388608\n"
      "W32_%=:\n"
      "ds_read_b32 v5, v3 offset:24\n"
      "s_sub_u32 s50, s50, 1\n"
      "s_cbranch_scc1 WD33_%=\n"
      "s_waitcnt lgkmcnt(0)\n"
      "v_cmp_gt_u32 vcc, 4, v5\n"
      "s_cbranch_vccnz W32_%=\n"
      "WD33_%=:\n"
      "ds_read_b128 v[44:47], v36 offset:0\n"
      "ds_read_b128 v[48:51], v36 offset:64\n"
      "s_mov_b32 s50, 524288\n"
      "s_waitcnt vmcnt(24)\n"
      "ds_read_b64_tr_b16 v[64:65], v37 offset:24576\n"
      "ds_read_b64_tr_b16 v[66:67], v37 offset:25088\n"
      "ds_read_b64_tr_b16 v[68:69], v37 offset:28672\n"
      "ds_read_b64_tr_b16 v[70:71], v37 offset:29184\n"
      "ds_read_b64_tr_b16 v[72:73], v40 offset:24576\n"
      "ds_read_b64_tr_b16 v[74:75], v40 offset:25088\n"
      "ds_read_b64_tr_b16 v[76:77], v40 offset:28672\n"
      "ds_read_b64_tr_b16 v[78:79], v40 offset:29184\n"
      "ds_read_b64_tr_b16 v[80:81], v41 offset:24576\n"
      "ds_read_b64_tr_b16 v[82:83], v41 offset:25088\n"
      "ds_read_b64_tr_b16 v[84:85], v41 offset:28672\n"
      "ds_read_b64_tr_b16 v[86:87], v41 offset:29184\n"
      "ds_read_b64_tr_b16 v[88:89], v42 offset:24576\n"
      "ds_read_b64_tr_b16 v[90:91], v42 offset:25088\n"
      "ds_read_b64_tr_b16 v[92:93], v42 offset:28672\n"
      "ds_read_b64_tr_b16 v[94:95], v42 offset:29184\n"
      "s_waitcnt lgkmcnt(12)\n"
      "v_mfma_f32_16x16x32_bf16 v[96:99], v[44:47], v[64:67], 0\n"
      "v_mfma_f32_16x16x32_bf16 v[96:99], v[48:51], v[68:71], v[96:99]\n"
      "s_waitcnt lgkmcnt(8)\n"
      "v_mfma_f32_16x16x32_bf16 v[100:103], v[44:47], v[72:75], 0\n"
      "v_mfma_f32_16x16x32_bf16 v[100:103], v[48:51], v[76:79], v[100:103]\n"
      "s_waitcnt lgkmcnt(4)\n"
      "v_mfma_f32_16x16x32_bf16 v[104:107], v[44:47], v[80:83], 0\n"
      "v_mfma_f32_16x16x32_bf16 v[104:107], v[48:51], v[84:87], v[104:107]\n"
      "s_waitcnt lgkmcnt(0)\n"
      "v_mfma_f32_16x16x32_bf16 v[108:111], v[44:47], v[88:91], 0\n"
      "v_mfma_f32_16x16x32_bf16 v[108:111], v[48:51], v[92:95], v[108:111]\n"
      "s_nop 1\n"
      "v_mov_b32_dpp v96, v100 quad_perm:[0,1,2,3] row_mask:0x2 bank_mask:0xf\n"
      "s_nop 1\n"
      "v_mov_b32_dpp v96, v104 quad_perm:[0,1,2,3] row_mask:0x4 bank_mask:0xf\n"
      "s_nop 1\n"
      "v_mov_b32_dpp v96, v108 quad_perm:[0,1,2,3] row_mask:0x8 bank_mask:0xf\n"
      "v_mov_b32 v97, s36\n"
      "buffer_store_dwordx2 v[96:97], v38, s[40:43], 0 offen sc1\n"
      "T34_%=:\n"
      "s_memrealtime s[6:7]\n"
      "s_waitcnt lgkmcnt(0)\n"
      "s_sub_u32 s6, s6, s4\n"
      "s_cmp_lt_i32 s6, 468\n"
      "s_cbranch_scc1 T34_%=\n"
      "buffer_load_dwordx4 v[52:55], v17, s[40:43], 0 offen sc1\n"
      "buffer_load_dwordx4 v[56:59], v17, s[40:43], 0 offen offset:16 sc1\n"
      "v_cmp_ne_u32 s[38:39], 7, v13\n"
      "s_setprio 3\n"
      "v_mov_b32 v25, 3\n"
      "s_waitcnt vmcnt(2)\n"
      "ds_write_b32 v3, v25 offset:16\n"
      "GC35_%=:\n"
      "s_waitcnt vmcnt(0)\n"
      "v_bitop3_b32 v20, v53, v55, s36 bitop3:0x7e\n"
      "v_bitop3_b32 v21, v57, v59, s36 bitop3:0x7e\n"
      "v_or_b32 v20, v20, v21\n"
      "v_cmp_ne_u32 vcc, 0, v20\n"
      "s_cbranch_vccnz GR37_%=\n"
      "GOK36_%=:\n"
      "v_permlane32_swap_b32 v52, v54\n"
      "v_permlane32_swap_b32 v56, v58\n"
      "v_add_f32 v58, v56, v58\n"
      "v_add_f32 v56, v52, v54\n"
      "v_lshlrev_b32 v14, 16, v43\n"
      "s_nop 0\n"
      "v_permlane16_swap_b32 v56, v58\n"
      "v_add_f32 v56, v56, v58\n"
      "s_nop 1\n"
      "v_add_f32_dpp v56, v56, v56 row_ror:8 row_mask:0xf bank_mask:0xf bound_ctrl:1\n"
      "v_cvt_pk_bf16_f32 v56, v56, v56\n"
      "v_lshlrev_b32 v56, 16, v56\n"
      "v_add_f32 v56, v56, v14\n"
      "v_cvt_pk_bf16_f32 v56, v56, v56\n"
      "v_lshlrev_b32 v56, 16, v56\n"
      "v_mul_f32 v57, 0x3d372713, v56\n"
      "v_mul_f32 v58, 0xc0135761, v56\n"
      "v_fma_f32 v57, v57, v56, 1.0\n"
      "v_mul_f32 v57, v57, v58\n"
      "v_exp_f32 v57, v57\n"
      "s_nop 0\n"
      "v_add_f32 v57, 1.0, v57\n"
      "v_rcp_f32 v57, v57\n"
      "s_nop 0\n"
      "v_mul_f32 v56, v56, v57\n"
      "v_cvt_pk_bf16_f32 v56, v56, v56\n"
      "v_cndmask_b32 v56, 0, v56, s[38:39]\n"
      "ds_write_b16 v19, v56 offset:64\n"
      "s_waitcnt lgkmcnt(0)\n"
      "s_endpgm\n"
      "GR37_%=:\n"
      "buffer_load_dwordx4 v[52:55], v17, s[40:43], 0 offen sc1\n"
      "buffer_load_dwordx4 v[56:59], v17, s[40:43], 0 offen offset:16 sc1\n"
      "s_sub_u32 s50, s50, 1\n"
      "s_cbranch_scc0 GC35_%=\n"
      "s_waitcnt vmcnt(0)\n"
      "s_branch GOK36_%=\n"
      "END_%=:\n"
      : "+{s3}"(lds_base)
      : "{s[0:1]}"(__builtin_amdgcn_kernarg_segment_ptr()), "{s2}"(blockIdx.x), "{v0}"(threadIdx.x)
      : "v1", "v2", "v3", "v4", "v5", "v6", "v7", "v8", "v9", "v10", "v11", "v12", "v13", "v14", "v15", "v16",
        "v17", "v18", "v19", "v20", "v21", "v22", "v23", "v24", "v25", "v26", "v27", "v28", "v29", "v30", "v31", "v32",
        "v33", "v34", "v35", "v36", "v37", "v38", "v39", "v40", "v41", "v42", "v43", "v44", "v45", "v46", "v47", "v48",
        "v49", "v50", "v51", "v52", "v53", "v54", "v55", "v56", "v57", "v58", "v59", "v60", "v61", "v62", "v63", "v64",
        "v65", "v66", "v67", "v68", "v69", "v70", "v71", "v72", "v73", "v74", "v75", "v76", "v77", "v78", "v79", "v80",
        "v81", "v82", "v83", "v84", "v85", "v86", "v87", "v88", "v89", "v90", "v91", "v92", "v93", "v94", "v95", "v96",
        "v97", "v98", "v99", "v100", "v101", "v102", "v103", "v104", "v105", "v106", "v107", "v108", "v109", "v110", "v111", "v112",
        "v113", "v114", "v115", "v116", "v117", "v118", "v119", "v120", "v121", "v122", "v123", "v124", "v125", "v126", "v127", "v128",
        "v129", "v130", "v131", "v132", "v133", "v134", "v135", "v136", "v137", "v138", "v139", "v140", "v141", "v142", "v143", "v144",
        "v145", "v146", "v147", "v148", "v149", "v150", "v151", "v152", "v153", "v154", "v155", "v156", "v157", "v158", "v159", "v160",
        "v161", "v162", "v163", "v164", "v165", "v166", "v167", "v168", "v169", "v170", "v171", "v172", "v173", "v174", "v175", "s4",
        "s5", "s6", "s7", "s8", "s9", "s10", "s11", "s12", "s13", "s14", "s15", "s16", "s17", "s18", "s19", "s20",
        "s21", "s22", "s23", "s24", "s25", "s26", "s27", "s28", "s29", "s30", "s31", "s33", "s34", "s35", "s36", "s37",
        "s38", "s39", "s40", "s41", "s42", "s43", "s44", "s45", "s46", "s47", "s48", "s49", "s50", "s51", "s52", "s53",
        "s54", "s55", "s56", "s57", "s58", "s59", "s60", "s61", "s62", "s63", "vcc", "scc", "memory");
}

extern "C" void kernel_launch(void* const* d_in, const int* in_sizes, int n_in, void* d_out, int out_size, void* d_ws, size_t ws_size,
                              hipStream_t stream) {
  const size_t win = (size_t)(32768 - 4096) * 128 * 2;
  hipLaunchKernelGGL(fused_decode, dim3(256), dim3(448), 0, stream, (const char*)d_in[3], (const char*)d_in[6], (const char*)d_in[1] + win,
                     (const char*)d_in[2] + win, (const char*)d_in[4], (const char*)d_in[8], (const char*)d_in[0], (char*)d_ws,
                     (const char*)d_in[7], (const char*)d_in[9], (char*)d_out);
}
```
